```python
import torch

def _softmax(x, axis=-1):
    return torch.softmax(x, dim=axis)
def _gelu(x):
    return torch.nn.functional.gelu(x, approximate="tanh")
def _silu(x):
    return torch.nn.functional.silu(x)
def _sigmoid(x):
    return torch.sigmoid(x)
def _layernorm(x, axis=-1, eps=1e-5):
    m = x.mean(dim=axis, keepdim=True)
    v = x.var(dim=axis, keepdim=True, unbiased=False)
    return (x - m) / torch.sqrt(v + eps)
def _scatter_add(tgt, idx, src, axis=0):
    return tgt.clone().index_add_(axis, idx, src)
def _segment_sum(src, seg, n):
    out = torch.zeros((n,) + src.shape[1:], dtype=src.dtype)
    return out.index_add_(0, seg, src)
def _pad(x, pw):
    flat = tuple(v for pair in reversed(pw) for v in pair)
    return torch.nn.functional.pad(x, flat)

kernel_name = 'concat_heads__linear_attn_block__ssm_conv_s4d__prompt'

def setup_inputs():

    _w = (lambda a: (lambda t: t.to(torch.bfloat16) if t.is_floating_point() else t)(torch.as_tensor(a)))
    def _A_setup():
        B, S, D = 1, 2048, 512
        return {
            "x": _w((lambda *s: torch.randn(*s, dtype=torch.bfloat16) * 0.05)(B, S, D)),
            "wqkv": _w((lambda *s: torch.randn(*s, dtype=torch.bfloat16) * 0.4)(D, 3 * D)),
            "wo": _w((lambda *s: torch.randn(*s, dtype=torch.bfloat16) * 0.05)(D, D)),
        }
    _A_in = _A_setup()

    def _B_setup():
        Bsz, L, D, N = 1, 2048, 512, 128
        return {
            "u": _w((lambda *s: torch.randn(*s, dtype=torch.bfloat16) * 0.05)(Bsz, L, D)),
            "log_A_real": _w((lambda *s: torch.randn(*s, dtype=torch.bfloat16) * 6.4)(D, N) * 0.5),
            "A_imag": _w((lambda *s: torch.randn(*s, dtype=torch.bfloat16) * 0.05)(D, N) * 3.14),
            "C_real": _w((lambda *s: torch.randn(*s, dtype=torch.bfloat16) * 0.1)(D, N) / N ** 0.5),
            "C_imag": _w((lambda *s: torch.randn(*s, dtype=torch.bfloat16) * 0.1)(D, N) / N ** 0.5),
            "log_dt": _w((lambda *s: torch.randn(*s, dtype=torch.bfloat16) * 0.05)(D) * 0.5 - 4.0),
            "D_skip": _w((lambda *s: torch.randn(*s, dtype=torch.bfloat16) * 0.05)(D)),
        }
    _B_in = _B_setup()

    return {
        "A_x": _A_in["x"],
        "A_wqkv": _A_in["wqkv"],
        "A_wo": _A_in["wo"],
        "B_u": _B_in["u"],
        "B_log_A_real": _B_in["log_A_real"],
        "B_A_imag": _B_in["A_imag"],
        "B_C_real": _B_in["C_real"],
        "B_C_imag": _B_in["C_imag"],
        "B_log_dt": _B_in["log_dt"],
        "B_D_skip": _B_in["D_skip"],
    }

def reference(A_x, A_wqkv, A_wo, B_u, B_log_A_real, B_A_imag, B_C_real, B_C_imag, B_log_dt, B_D_skip):
    def _A_ref(x, wqkv, wo):
        B, S, D = x.shape
        H = 32; Dh = D // H
        qkv = (lambda x, *d: x.permute(*d))((x @ wqkv).reshape(B, S, 3, H, Dh), 2, 0, 3, 1, 4)
        q, k, v = qkv[0], qkv[1], qkv[2]
        phi = lambda t: torch.where(t > 0, t + 1, torch.exp(t)) + 1e-6
        pq, pk = phi(q), phi(k)
        kv = torch.einsum("bhsd,bhse->bhde", pk, v)
        z = torch.einsum("bhsd->bhd", pk)
        num = torch.einsum("bhqd,bhde->bhqe", pq, kv)
        den = torch.einsum("bhqd,bhd->bhq", pq, z)[..., None] + 1e-6
        o = num / den
        return (lambda x, *d: x.permute(*d))(o, 0, 2, 1, 3).reshape(B, S, D) @ wo

    def _B_ref(u, log_A_real, A_imag, C_real, C_imag, log_dt, D_skip):
        L = u.shape[1]
        dt = torch.exp(log_dt)
        A = -torch.exp(log_A_real) + 1j * A_imag
        C = (C_real + 1j * C_imag) * dt[:, None]
        pos = (lambda x, dt: x.to(dt))(torch.arange(L), log_dt.dtype)
        P = torch.exp((A * dt[:, None])[:, :, None] * pos[None, None, :])
        K = 2.0 * torch.einsum("dn,dnl->dl", C, P).real
        L2 = 2 * L
        u_t = (lambda x, *d: x.permute(*d))(u, 0, 2, 1)
        Kf = (lambda x, *a, **k: torch.fft.rfft(x.float(), *a, **k))(K, L2)
        Uf = (lambda x, *a, **k: torch.fft.rfft(x.float(), *a, **k))(u_t, L2)
        y = (lambda x, *a, **k: torch.fft.irfft(x, *a, **k).to(torch.bfloat16))(Uf * Kf[None, :, :], L2)[:, :, :L]
        y = (lambda x, *d: x.permute(*d))(y, 0, 2, 1)
        return y + u * D_skip[None, None, :]

    a = _A_ref(A_x, A_wqkv, A_wo)
    b = _B_ref(B_u, B_log_A_real, B_A_imag, B_C_real, B_C_imag, B_log_dt, B_D_skip)
    return torch.concatenate([a, b], axis=-1)
```

```cpp
#include <hip/hip_runtime.h>
#include <hip/hip_bf16.h>
#include <stdint.h>
#define ROUND_POS 1
typedef unsigned short u16;
__device__ __forceinline__ float bf2f(u16 v){ return __uint_as_float(((unsigned)v)<<16); }
__device__ __forceinline__ u16 f2bf(float f){ unsigned u=__float_as_uint(f); u += 0x7FFF + ((u>>16)&1); return (u16)(u>>16); }
__device__ __forceinline__ float rbf(float f){ return bf2f(f2bf(f)); }
__device__ __forceinline__ float phi(float t){ float a = t>0.f ? rbf(t+1.f) : rbf(expf(t)); return rbf(a+1e-6f); }

__global__ void k_qkv(const u16* x, const u16* w, float* qkv){
  int n = blockIdx.x*256+threadIdx.x; int s = blockIdx.y;
  float acc=0.f;
  for(int k=0;k<512;k++) acc += bf2f(x[s*512+k])*bf2f(w[k*1536+n]);
  acc = rbf(acc);
  if(n<1024) acc = phi(acc);
  qkv[s*1536+n]=acc;
}
__global__ void k_kv(const float* qkv, float* kv, float* z){
  int h = blockIdx.x; int d = threadIdx.x>>4, e = threadIdx.x&15;
  float acc=0.f, zz=0.f;
  for(int s=0;s<2048;s++){ float pk=qkv[s*1536+512+h*16+d]; acc += pk*qkv[s*1536+1024+h*16+e]; zz+=pk; }
  kv[h*256+d*16+e]=rbf(acc);
  if(e==0) z[h*16+d]=rbf(zz);
}
__global__ void k_o(const float* qkv, const float* kv, const float* z, float* o){
  int s = blockIdx.x; int c = threadIdx.x; int h=c>>4, e=c&15;
  float num=0.f, den=0.f;
  for(int d=0;d<16;d++){ float pq=qkv[s*1536+h*16+d]; num+=pq*kv[h*256+d*16+e]; den+=pq*z[h*16+d]; }
  num=rbf(num); den=rbf(rbf(den)+1e-6f);
  o[s*512+c]=rbf(num/den);
}
__global__ void k_out(const float* o, const u16* wo, u16* out){
  int n = blockIdx.x*256+threadIdx.x; int s = blockIdx.y;
  float acc=0.f;
  for(int k=0;k<512;k++) acc += o[s*512+k]*bf2f(wo[k*512+n]);
  out[s*1024+n]=f2bf(acc);
}
__global__ void k_kgen(const u16* lar,const u16* ai,const u16* cr,const u16* ci,const u16* ldt,float* K){
  int d = blockIdx.y; int l = blockIdx.x*256+threadIdx.x;
  float dt = rbf(expf(bf2f(ldt[d])));
  float pos = ROUND_POS ? rbf((float)l) : (float)l;
  float acc=0.f;
  for(int n=0;n<128;n++){
    float ar = -rbf(expf(bf2f(lar[d*128+n])))*dt;
    float im = bf2f(ai[d*128+n])*dt;
    float c_r = bf2f(cr[d*128+n])*dt, c_i = bf2f(ci[d*128+n])*dt;
    float m = expf(ar*pos); float sn, cs; sincosf(im*pos,&sn,&cs);
    acc += c_r*(m*cs) - c_i*(m*sn);
  }
  K[d*2048+l]=2.f*acc;
}
__global__ void k_conv(const float* K,const u16* u,const u16* dsk,u16* out){
  int d = blockIdx.x*64+(threadIdx.x&63); int t = blockIdx.y*4+(threadIdx.x>>6);
  float acc=0.f;
  for(int l=0;l<=t;l++) acc += K[d*2048+l]*bf2f(u[(t-l)*512+d]);
  float y = rbf(acc);
  float sk = rbf(bf2f(u[t*512+d])*bf2f(dsk[d]));
  out[t*1024+512+d]=f2bf(y+sk);
}
extern "C" void kernel_launch(void* const* d_in, const int* in_sizes, int n_in,
                              void* d_out, int out_size, void* d_ws, size_t ws_size,
                              hipStream_t stream) {
  const u16* x=(const u16*)d_in[0]; const u16* wqkv=(const u16*)d_in[1]; const u16* wo=(const u16*)d_in[2];
  const u16* u=(const u16*)d_in[3]; const u16* lar=(const u16*)d_in[4]; const u16* ai=(const u16*)d_in[5];
  const u16* cr=(const u16*)d_in[6]; const u16* ci=(const u16*)d_in[7]; const u16* ldt=(const u16*)d_in[8]; const u16* dsk=(const u16*)d_in[9];
  u16* out=(u16*)d_out;
  float* ws=(float*)d_ws;
  float* qkv=ws; float* kv=qkv+2048*1536; float* z=kv+32*256; float* o=z+512; float* K=o+2048*512;
  k_qkv<<<dim3(6,2048),256,0,stream>>>(x,wqkv,qkv);
  k_kv<<<32,256,0,stream>>>(qkv,kv,z);
  k_o<<<2048,512,0,stream>>>(qkv,kv,z,o);
  k_out<<<dim3(2,2048),256,0,stream>>>(o,wo,out);
  k_kgen<<<dim3(8,512),256,0,stream>>>(lar,ai,cr,ci,ldt,K);
  k_conv<<<dim3(8,512),256,0,stream>>>(K,u,dsk,out);
}
```

```cpp
#include <hip/hip_runtime.h>
#include <stdint.h>

#define LAS __attribute__((address_space(3)))
#define GAS __attribute__((address_space(1)))
#define DEV __device__ __forceinline__
typedef unsigned short u16;
typedef unsigned long long u64;
typedef unsigned v4u __attribute__((ext_vector_type(4)));
typedef unsigned v2u __attribute__((ext_vector_type(2)));
typedef short s16x4 __attribute__((ext_vector_type(4)));
typedef __bf16 bf16x8 __attribute__((ext_vector_type(8)));
typedef __bf16 bf16x2 __attribute__((ext_vector_type(2)));
typedef _Float16 f16x2 __attribute__((ext_vector_type(2)));
typedef _Float16 f16x8 __attribute__((ext_vector_type(8)));
typedef float f32x4 __attribute__((ext_vector_type(4)));
typedef float f32x2 __attribute__((ext_vector_type(2)));
typedef GAS u64 gu64;

#define STAMPS 0

constexpr int FL = 128;
constexpr int WS_EPA = 0;
constexpr int WS_EPB = 256;
constexpr int WS_FKV = 4096;
constexpr int WS_FUT = WS_FKV + 256 * FL;
constexpr int WS_FY = WS_FUT + 256 * FL;
constexpr int WS_FO = WS_FY + 256 * FL;
constexpr int KVP_REC = 2304;
constexpr int WS_KVP = WS_FO + 1024 * FL;
constexpr int WS_O = WS_KVP + 256 * KVP_REC;
constexpr int WS_UT = WS_O + 2 * 1024 * 1024;
constexpr int WS_YT = WS_UT + 2 * 1024 * 1024;
constexpr int WS_STAMP = WS_YT + 2 * 1024 * 1024;
constexpr int WS_BYTES = WS_STAMP + 512 * 4 * 16 * 8;
constexpr int LDS_BYTES = 80 * 1024;
constexpr unsigned SPIN_CAP = 1u << 18;

struct Params {
  const u16 *x, *wqkv, *wo, *u, *lar, *aim, *cre, *cim, *ldt, *dsk;
  u16* out;
  unsigned char* ws;
};

DEV float bf2f(unsigned v) { return __uint_as_float(v << 16); }
DEV unsigned pk_bf16(float a, float b) {
  f32x2 v = {a, b};
  return __builtin_bit_cast(unsigned, __builtin_convertvector(v, bf16x2));
}
DEV unsigned pk_f16(float a, float b) {
  f32x2 v = {a, b};
  return __builtin_bit_cast(unsigned, __builtin_convertvector(v, f16x2));
}
DEV float rbf(float a) { return __uint_as_float(pk_bf16(a, a) << 16); }
DEV f32x4 mfma_bf16(v4u a, v4u b, f32x4 c) {
  return __builtin_amdgcn_mfma_f32_16x16x32_bf16(__builtin_bit_cast(bf16x8, a), __builtin_bit_cast(bf16x8, b), c, 0, 0, 0);
}
DEV f32x4 mfma_f16(v4u a, v4u b, f32x4 c) {
  return __builtin_amdgcn_mfma_f32_16x16x32_f16(__builtin_bit_cast(f16x8, a), __builtin_bit_cast(f16x8, b), c, 0, 0, 0);
}
DEV f32x4 mfma16_bf16(v2u a, v2u b, f32x4 c) {
  return __builtin_amdgcn_mfma_f32_16x16x16bf16_1k(__builtin_bit_cast(s16x4, a), __builtin_bit_cast(s16x4, b), c, 0, 0, 0);
}
DEV v2u tr_read(LAS unsigned char* p) {
  return __builtin_bit_cast(v2u, __builtin_amdgcn_ds_read_tr16_b64_v4i16((LAS s16x4*)p));
}
DEV v4u tr_read2(LAS unsigned char* p0, LAS unsigned char* p1) {
  v2u a = tr_read(p0), b = tr_read(p1);
  return v4u{a.x, a.y, b.x, b.y};
}
DEV void glds16(const void* sbase, unsigned voff, unsigned lds_dst) {
  unsigned keep;
  asm volatile("s_mov_b32 %0, m0\n\ts_mov_b32 m0, %3\n\ts_nop 0\n\tglobal_load_lds_dwordx4 %1, %2\n\ts_mov_b32 m0, %0"
               : "=&s"(keep) : "v"(voff), "s"(sbase), "s"(lds_dst) : "memory");
}
#define DRAIN() asm volatile("s_waitcnt vmcnt(0)" ::: "memory")
#define BAR() do { asm volatile("s_waitcnt lgkmcnt(0)" ::: "memory"); __builtin_amdgcn_s_barrier(); asm volatile("" ::: "memory"); } while (0)
#define RLX_AGENT __ATOMIC_RELAXED, __HIP_MEMORY_SCOPE_AGENT
constexpr int AUX_SC1 = 16;
typedef __amdgpu_buffer_rsrc_t rsrc_t;
DEV v4u ld_sc1(rsrc_t rs, int off) { return __builtin_amdgcn_raw_buffer_load_b128(rs, off, 0, AUX_SC1); }
DEV void st_sc1(rsrc_t rs, int off, v4u v) { __builtin_amdgcn_raw_buffer_store_b128(v, rs, off, 0, AUX_SC1); }
DEV void wait_flags(unsigned char* ws, int off, bool active, u64 tag) {
  gu64* f = (gu64*)(ws + off);
  for (unsigned spins = 0; spins < SPIN_CAP; ++spins) {
    bool ok = true;
    if (active) ok = __hip_atomic_load(f, RLX_AGENT) == tag;
    if (__all(ok)) break;
    __builtin_amdgcn_s_sleep(1);
  }
  __builtin_amdgcn_fence(__ATOMIC_ACQUIRE, "wavefront");
}
DEV void set_flag(unsigned char* ws, int off, u64 tag) { __hip_atomic_store((gu64*)(ws + off), tag, RLX_AGENT); }

#define STAMP(k) do { } while (0)

DEV float phi(float t) {
  float e = __builtin_amdgcn_exp2f(t * 1.44269504088896341f);
  e = __builtin_amdgcn_fmed3f(e, 0.f, 1.f);
  return (fmaxf(t, 0.f) + e) + 1e-6f;
}
DEV f32x2 cmul(f32x2 a, f32x2 b) {
  f32x2 t = a.xx * b;
  f32x2 n = {-a.y, a.y};
  return __builtin_elementwise_fma(n, b.yx, t);
}

DEV void roleA(const Params& p, LAS unsigned char* L, const int a) {
  const int tid = threadIdx.x, lane = tid & 63, w = __builtin_amdgcn_readfirstlane(tid >> 6);
  const int lr = lane & 15, lq = lane >> 4;
  const int g = a & 7, m = a >> 3;
  const int bi = 4 * (g >> 1) + (m >> 3), bj = 8 * (g & 1) + (m & 7);
  const unsigned Lb = (unsigned)(uintptr_t)L;
  const rsrc_t rs = __builtin_amdgcn_make_buffer_rsrc(p.ws, 0, WS_BYTES, 0x00020000);
  STAMP(0);
  const unsigned wvoff = (unsigned)(((lane >> 2) * 1536 + 32 * bj + 8 * ((lane & 3) ^ ((lane >> 5) << 1))) * 2);
#pragma unroll
  for (int n = 0; n < 8; ++n) {
#pragma unroll
    for (int sg = 0; sg < 2; ++sg)
      glds16(p.wqkv + (size_t)(128 * w + 16 * n) * 1536 + 512 * (1 + sg), wvoff, Lb + sg * 32768 + (128 * w + 16 * n) * 64);
  }
  v4u xf[2][16];
  {
    const GAS v4u* xp = (const GAS v4u*)(p.x + (size_t)(128 * bi + 32 * w + lr) * 512 + 8 * lq);
#pragma unroll
    for (int ks = 0; ks < 16; ++ks) {
      xf[0][ks] = xp[ks * 4];
      xf[1][ks] = xp[ks * 4 + 1024];
    }
  }
  v4u wq[8];
  {
    const GAS unsigned char* qp = (const GAS unsigned char*)(p.wqkv + (size_t)(128 * w) * 1536) + wvoff;
#pragma unroll
    for (int n = 0; n < 8; ++n) wq[n] = *(const GAS v4u*)(qp + (size_t)n * 16 * 3072);
  }
  const u64 tag = __hip_atomic_load((gu64*)(p.ws + WS_EPA), RLX_AGENT) + 1;
  __builtin_amdgcn_sched_barrier(0);
  asm volatile("" ::"v"(xf[0][0]));
  BAR();
  STAMP(1);
  LAS unsigned char* tb0 = L + (8 * lq + ((lane >> 2) & 3)) * 64 + 32 * (lq & 1) + 8 * (lane & 3);
  LAS unsigned char* tb1 = L + (8 * lq + ((lane >> 2) & 3)) * 64 + 32 * ((lq & 1) ^ 1) + 8 * (lane & 3);
  f32x4 ak[2][2], av[2][2];
#pragma unroll
  for (int r = 0; r < 2; ++r)
#pragma unroll
    for (int h = 0; h < 2; ++h) { ak[r][h] = f32x4{0, 0, 0, 0}; av[r][h] = f32x4{0, 0, 0, 0}; }
#pragma unroll
  for (int ks = 0; ks < 16; ++ks) {
    const v4u k0 = tr_read2(tb0 + ks * 2048, tb0 + ks * 2048 + 256);
    const v4u k1 = tr_read2(tb1 + ks * 2048, tb1 + ks * 2048 + 256);
    const v4u v0 = tr_read2(tb0 + 32768 + ks * 2048, tb0 + 32768 + ks * 2048 + 256);
    const v4u v1 = tr_read2(tb1 + 32768 + ks * 2048, tb1 + 32768 + ks * 2048 + 256);
#pragma unroll
    for (int r = 0; r < 2; ++r) {
      ak[r][0] = mfma_bf16(xf[r][ks], k0, ak[r][0]);
      ak[r][1] = mfma_bf16(xf[r][ks], k1, ak[r][1]);
      av[r][0] = mfma_bf16(xf[r][ks], v0, av[r][0]);
      av[r][1] = mfma_bf16(xf[r][ks], v1, av[r][1]);
    }
  }
  STAMP(2);
  LAS unsigned char* S = L + 65536;
  {
    const v4u ones = {0x3F803F80u, 0x3F803F80u, 0x3F803F80u, 0x3F803F80u};
#pragma unroll
    for (int h = 0; h < 2; ++h) {
      v4u pk, vv;
      pk.x = pk_bf16(phi(ak[0][h][0]), phi(ak[0][h][1]));
      pk.y = pk_bf16(phi(ak[0][h][2]), phi(ak[0][h][3]));
      pk.z = pk_bf16(phi(ak[1][h][0]), phi(ak[1][h][1]));
      pk.w = pk_bf16(phi(ak[1][h][2]), phi(ak[1][h][3]));
      vv.x = pk_bf16(av[0][h][0], av[0][h][1]);
      vv.y = pk_bf16(av[0][h][2], av[0][h][3]);
      vv.z = pk_bf16(av[1][h][0], av[1][h][1]);
      vv.w = pk_bf16(av[1][h][2], av[1][h][3]);
      const f32x4 kv = mfma_bf16(pk, vv, f32x4{0, 0, 0, 0});
      const f32x4 zz = mfma_bf16(pk, ones, f32x4{0, 0, 0, 0});
      *(LAS f32x4*)(S + w * 2176 + h * 1024 + lane * 16) = kv;
      if (lr == 0) *(LAS f32x4*)(S + w * 2176 + 2048 + h * 64 + lq * 16) = zz;
    }
  }
  asm volatile("" ::"v"(tag));
  BAR();
  if (w == 0) {
    const int rec = WS_KVP + (bj * 16 + bi) * KVP_REC;
    f32x4 s0 = *(LAS f32x4*)(S + lane * 16), s1 = *(LAS f32x4*)(S + 1024 + lane * 16);
    f32x4 sz = *(LAS f32x4*)(S + 2048 + (lane & 7) * 16);
#pragma unroll
    for (int ww = 1; ww < 4; ++ww) {
      s0 += *(LAS f32x4*)(S + ww * 2176 + lane * 16);
      s1 += *(LAS f32x4*)(S + ww * 2176 + 1024 + lane * 16);
      sz += *(LAS f32x4*)(S + ww * 2176 + 2048 + (lane & 7) * 16);
    }
    st_sc1(rs, rec + lane * 16, __builtin_bit_cast(v4u, s0));
    st_sc1(rs, rec + 1024 + lane * 16, __builtin_bit_cast(v4u, s1));
    if (lane < 8) st_sc1(rs, rec + 2048 + lane * 16, __builtin_bit_cast(v4u, sz));
    DRAIN();
    if (lane == 0) set_flag(p.ws, WS_FKV + (bj * 16 + bi) * FL, tag);
  }
  STAMP(3);
  const int R = 4 * g + (m >> 3), C = m & 7;
  const unsigned ovoff = (unsigned)(((lane >> 3) * 512 + 64 * C + 8 * ((lane & 7) ^ (((lane >> 4) & 3) << 1))) * 2);
#pragma unroll
  for (int n = 0; n < 8; ++n) *(LAS v4u*)(L + (128 * w + 16 * n) * 64 + lane * 16) = wq[n];
#pragma unroll
  for (int n = 0; n < 8; ++n) glds16(p.wo + (size_t)(64 * w + 8 * n) * 512, ovoff, Lb + 32768 + (64 * w + 8 * n) * 128);
  BAR();
  f32x4 aq[2][2];
#pragma unroll
  for (int r = 0; r < 2; ++r)
#pragma unroll
    for (int h = 0; h < 2; ++h) aq[r][h] = f32x4{0, 0, 0, 0};
#pragma unroll
  for (int ks = 0; ks < 16; ++ks) {
    const v4u q0 = tr_read2(tb0 + ks * 2048, tb0 + ks * 2048 + 256);
    const v4u q1 = tr_read2(tb1 + ks * 2048, tb1 + ks * 2048 + 256);
#pragma unroll
    for (int r = 0; r < 2; ++r) {
      aq[r][0] = mfma_bf16(q0, xf[r][ks], aq[r][0]);
      aq[r][1] = mfma_bf16(q1, xf[r][ks], aq[r][1]);
    }
  }
  v2u pq[2][2];
#pragma unroll
  for (int r = 0; r < 2; ++r)
#pragma unroll
    for (int h = 0; h < 2; ++h) {
      pq[r][h].x = pk_bf16(phi(aq[r][h][0]), phi(aq[r][h][1]));
      pq[r][h].y = pk_bf16(phi(aq[r][h][2]), phi(aq[r][h][3]));
    }
  STAMP(4);
  wait_flags(p.ws, WS_FKV + (bj * 16 + 4 * w + (lane & 3)) * FL, lane < 4, tag);
  STAMP(5);
  {
    v4u t0[4], t1[4], tz[4];
#pragma unroll
    for (int q = 0; q < 4; ++q) {
      const int rec = WS_KVP + (bj * 16 + 4 * w + q) * KVP_REC;
      t0[q] = ld_sc1(rs, rec + lane * 16);
      t1[q] = ld_sc1(rs, rec + 1024 + lane * 16);
      tz[q] = ld_sc1(rs, rec + 2048 + (lane & 7) * 16);
    }
    f32x4 s0 = __builtin_bit_cast(f32x4, t0[0]), s1 = __builtin_bit_cast(f32x4, t1[0]), sz = __builtin_bit_cast(f32x4, tz[0]);
#pragma unroll
    for (int q = 1; q < 4; ++q) {
      s0 += __builtin_bit_cast(f32x4, t0[q]);
      s1 += __builtin_bit_cast(f32x4, t1[q]);
      sz += __builtin_bit_cast(f32x4, tz[q]);
    }
    *(LAS f32x4*)(S + w * 2176 + lane * 16) = s0;
    *(LAS f32x4*)(S + w * 2176 + 1024 + lane * 16) = s1;
    if (lane < 8) *(LAS f32x4*)(S + w * 2176 + 2048 + lane * 16) = sz;
  }
  BAR();
  STAMP(6);
  {
    v2u kvb[2], zb[2];
#pragma unroll
    for (int h = 0; h < 2; ++h) {
      f32x4 s = *(LAS f32x4*)(S + h * 1024 + lane * 16);
      f32x4 z = *(LAS f32x4*)(S + 2048 + h * 64 + lq * 16);
#pragma unroll
      for (int ww = 1; ww < 4; ++ww) {
        s += *(LAS f32x4*)(S + ww * 2176 + h * 1024 + lane * 16);
        z += *(LAS f32x4*)(S + ww * 2176 + 2048 + h * 64 + lq * 16);
      }
      kvb[h] = v2u{pk_bf16(s[0], s[1]), pk_bf16(s[2], s[3])};
      zb[h] = v2u{pk_bf16(z[0], z[1]), pk_bf16(z[2], z[3])};
    }
#pragma unroll
    for (int r = 0; r < 2; ++r) {
      v4u ov;
#pragma unroll
      for (int h = 0; h < 2; ++h) {
        const f32x4 num = mfma16_bf16(kvb[h], pq[r][h], f32x4{0, 0, 0, 0});
        const f32x4 den = mfma16_bf16(zb[h], pq[r][h], f32x4{0, 0, 0, 0});
        const float rd = __builtin_amdgcn_rcpf(den[0] + 1e-6f);
        const unsigned lo = pk_bf16(num[0] * rd, num[1] * rd), hi = pk_bf16(num[2] * rd, num[3] * rd);
        if (h == 0) { ov.x = lo; ov.y = hi; } else { ov.z = lo; ov.w = hi; }
      }
      st_sc1(rs, WS_O + ((8 * bi + 2 * w + r) * 16 + bj) * 1024 + lane * 16, ov);
    }
    DRAIN();
    if (lane == 0) set_flag(p.ws, WS_FO + ((bi * 4 + w) * 16 + bj) * FL, tag);
  }
  STAMP(7);
#pragma unroll
  for (int n = 0; n < 8; ++n) glds16(p.wo + (size_t)(256 + 64 * w + 8 * n) * 512, ovoff, Lb + (64 * w + 8 * n) * 128);
  const int T = 4 * R + w;
  wait_flags(p.ws, WS_FO + ((T >> 1) * 16 + lr) * FL, lane < 16, tag);
  STAMP(8);
  v4u of[16];
#pragma unroll
  for (int j = 0; j < 16; ++j) of[j] = ld_sc1(rs, WS_O + (T * 16 + j) * 1024 + lane * 16);
  if (a == 0 && tid == 0) set_flag(p.ws, WS_EPA, tag);
  __builtin_amdgcn_sched_barrier(0);
  asm volatile("" ::"v"(of[0]));
  BAR();
  STAMP(9);
  f32x4 acc[4];
#pragma unroll
  for (int c = 0; c < 4; ++c) acc[c] = f32x4{0, 0, 0, 0};
  {
    const int ra = 4 * lq + ((lane >> 2) & 3);
    const int sw = (ra >> 1) & 3;
    LAS unsigned char* ob = L + 128 * ra + 8 * (lane & 3);
#pragma unroll
    for (int j = 0; j < 16; ++j) {
      const int off = (j < 8) ? 32768 + j * 4096 : (j - 8) * 4096;
#pragma unroll
      for (int c = 0; c < 4; ++c) {
        LAS unsigned char* q = ob + 32 * (c ^ sw) + off;
        const v4u wf = tr_read2(q, q + 2048);
        acc[c] = mfma_bf16(wf, of[j], acc[c]);
      }
    }
  }
  STAMP(10);
  {
    GAS unsigned char* op = (GAS unsigned char*)(p.out + (size_t)(16 * T + lr) * 1024 + 64 * C) + (lq & 1) * 32 + (lq >> 1) * 16;
#pragma unroll
    for (int c = 0; c < 4; c += 2) {
      unsigned a0 = pk_bf16(acc[c][0], acc[c][1]), a1 = pk_bf16(acc[c][2], acc[c][3]);
      unsigned b0 = pk_bf16(acc[c + 1][0], acc[c + 1][1]), b1 = pk_bf16(acc[c + 1][2], acc[c + 1][3]);
      auto r0 = __builtin_amdgcn_permlane16_swap(a0, b0, false, false);
      auto r1 = __builtin_amdgcn_permlane16_swap(a1, b1, false, false);
      *(GAS v4u*)(op + c * 32) = v4u{r0[0], r1[0], r0[1], r1[1]};
    }
  }
  STAMP(11);
}

constexpr int TROW = 528;
constexpr int TAB_CH = 57 * TROW;
constexpr int B_TAB = 0;
constexpr int B_KX = 60416;
constexpr int KX_CH = 4352;
constexpr int B_U = B_KX + 2 * KX_CH;
constexpr int U_CH = 4608;
constexpr int CPY = 4160;
constexpr int CPY_CH = 4 * CPY;
static_assert(B_U + 2 * U_CH <= LDS_BYTES, "lds");

DEV f32x2 cpow_direct(float al, float be, float pw) {
  const float mag = __builtin_amdgcn_exp2f(al * pw);
  const float ph = __builtin_amdgcn_fractf(be * pw);
  return f32x2{mag * __builtin_amdgcn_cosf(ph), mag * __builtin_amdgcn_sinf(ph)};
}

DEV void roleB(const Params& p, LAS unsigned char* L, const int b) {
  const int tid = threadIdx.x, lane = tid & 63, w = __builtin_amdgcn_readfirstlane(tid >> 6);
  const int lr = lane & 15, lq = lane >> 4;
  const int g = b & 7, m = b >> 3;
  const int ch = w >> 1;
  const int d = 64 * g + 2 * m + ch;
  const rsrc_t rs = __builtin_amdgcn_make_buffer_rsrc(p.ws, 0, WS_BYTES, 0x00020000);
  STAMP(0);
  const GAS v4u* up = (const GAS v4u*)(p.u + (size_t)(64 * m + (tid >> 3)) * 512 + 64 * g + 8 * (tid & 7));
  const v4u ut0 = up[0], ut1 = up[32 * 64];
  const u64 tag = __hip_atomic_load((gu64*)(p.ws + WS_EPB + 256 * g), RLX_AGENT) + 1;
  const int n = tid & 127;
  const unsigned r_lar = p.lar[d * 128 + n], r_aim = p.aim[d * 128 + n];
  const unsigned r_cre = p.cre[d * 128 + n], r_cim = p.cim[d * 128 + n];
  const unsigned r_cre2 = p.cre[d * 128 + (n ^ 64)], r_cim2 = p.cim[d * 128 + (n ^ 64)];
  const unsigned r_ldt = p.ldt[d], r_dsk = p.dsk[d];
  *(LAS v4u*)(L + (tid >> 3) * 128 + (tid & 7) * 16) = ut0;
  *(LAS v4u*)(L + 4096 + (tid >> 3) * 128 + (tid & 7) * 16) = ut1;
  if (tid < 64) *(LAS v4u*)(L + B_U + (tid >> 5) * U_CH + (tid & 31) * 16) = v4u{0, 0, 0, 0};
  else if (tid < 68) *(LAS v4u*)(L + B_KX + ((tid >> 1) & 1) * KX_CH + (tid & 1) * 16) = v4u{0, 0, 0, 0};
  BAR();
  STAMP(1);
  {
    LAS unsigned char* q = L + (8 * lq + ((lane >> 2) & 3)) * 128 + 32 * w + 8 * (lane & 3);
#pragma unroll
    for (int it = 0; it < 2; ++it) {
      const v4u v = tr_read2(q + it * 4096, q + it * 4096 + 512);
      const int quad = 16 * g + 4 * w + (lr >> 2), oct = 8 * m + 4 * it + lq;
      st_sc1(rs, WS_UT + (quad * 256 + oct) * 64 + (lr & 3) * 16, v);
    }
    DRAIN();
  }
  BAR();
  if (tid == 0) set_flag(p.ws, WS_FUT + (g * 32 + m) * FL, tag);
  STAMP(2);
  float kscale;
  {
    const float dt = rbf(expf(bf2f(r_ldt)));
    const float ar = -rbf(expf(bf2f(r_lar))) * dt;
    const float al = ar * 1.44269504088896341f;
    const float be = (bf2f(r_aim) * dt) * 0.15915494309189535f;
    unsigned mx = max(max(r_cre & 0x7FFFu, r_cim & 0x7FFFu), max(r_cre2 & 0x7FFFu, r_cim2 & 0x7FFFu));
#pragma unroll
    for (int o = 32; o >= 1; o >>= 1) mx = max(mx, (unsigned)__shfl_xor((int)mx, o));
    int ex = (int)(mx >> 7);
    ex = min(max(ex, 1), 253);
    const float cs = __uint_as_float((unsigned)(254 - ex) << 23);
    kscale = 2.f * dt * __uint_as_float((unsigned)ex << 23);
    const f32x2 z1 = cpow_direct(al, be, 1.f), z8 = cpow_direct(al, be, 8.f), z128 = cpow_direct(al, be, 128.f);
    const f32x2 z1024 = cpow_direct(al, be, 1024.f);
    LAS unsigned char* tp = L + B_TAB + ch * TAB_CH + n * 4;
#define PUTG(row, v) *(LAS unsigned*)(tp + (row) * TROW) = pk_f16((v).x, (v).y)
    f32x2 zp[8];
    zp[0] = f32x2{1.f, 0.f};
    zp[1] = z1;
#pragma unroll
    for (int i = 2; i < 8; ++i) zp[i] = cmul(zp[i - 1], z1);
#pragma unroll
    for (int i = 0; i < 8; ++i) PUTG(i, zp[i]);
    f32x2 f = f32x2{bf2f(r_cre) * cs, bf2f(r_cim) * cs};
    f32x2 h = z128, h8 = z1024;
#pragma unroll
    for (int i = 0; i < 16; ++i) {
      *(LAS unsigned*)(tp + (41 + i) * TROW) = pk_f16(f.x, -f.y);
      if (i < 15) f = cmul(f, z8);
      if (i < 9) {
        PUTG(32 + i, h8);
        if (i < 8) h8 = cmul(h8, z128);
      }
      if (i >= 1 && i <= 7) {
        if (i == 1) {
          PUTG(8, h);
#pragma unroll
          for (int q = 1; q < 8; ++q) PUTG(8 + q, cmul(h, zp[q]));
        } else if (i <= 3) {
          PUTG(16 + 4 * (i - 2), h);
#pragma unroll
          for (int q = 1; q < 4; ++q) PUTG(16 + 4 * (i - 2) + q, cmul(h, zp[2 * q]));
        } else {
          PUTG(24 + 2 * (i - 4), h);
          PUTG(24 + 2 * (i - 4) + 1, cmul(h, zp[4]));
        }
        if (i < 7) h = cmul(h, z128);
      }
    }
#undef PUTG
  }
  BAR();
  STAMP(3);
  {
    LAS unsigned char* tb = L + B_TAB + ch * TAB_CH + lq * 16;
    LAS unsigned char* kx = L + B_KX + ch * KX_CH + 32;
    LAS unsigned char* fb = tb + (41 + lr) * TROW;
    if ((w & 1) == 0) {
      LAS unsigned char* g0 = tb + lr * TROW;
      LAS unsigned char* g2 = tb + (32 + min(lr, 8)) * TROW;
      f32x4 c0 = {0, 0, 0, 0}, c2 = {0, 0, 0, 0};
#pragma unroll
      for (int s = 0; s < 8; ++s) {
        const v4u fv = *(LAS v4u*)(fb + s * 64);
        c0 = mfma_f16(*(LAS v4u*)(g0 + s * 64), fv, c0);
        c2 = mfma_f16(*(LAS v4u*)(g2 + s * 64), fv, c2);
      }
      c0 *= kscale;
      c2 *= kscale;
      *(LAS v2u*)(kx + 2 * (128 * (lq >> 1) + 8 * lr + 4 * (lq & 1))) = v2u{pk_bf16(c0[0], c0[1]), pk_bf16(c0[2], c0[3])};
#pragma unroll
      for (int r = 0; r < 4; ++r) {
        const unsigned vv = pk_bf16(c2[r], c2[r]);
        const unsigned pv = (unsigned)__builtin_amdgcn_update_dpp(0, (int)vv, 0x111, 0xf, 0xf, false);
        const unsigned first = (lr & 1) ? ((pv & 0xFFFFu) | (vv & 0xFFFF0000u)) : vv;
        const int pp = 128 * (8 + 4 * lq + r) + 8 * lr;
        if (lq < 2) {
          if (pp != 1024) *(LAS unsigned*)(kx + 2 * (pp - 4)) = first;
          *(LAS unsigned*)(kx + 2 * (pp - 2)) = vv;
          *(LAS v2u*)(kx + 2 * pp) = v2u{vv, vv};
        } else if (pp == 2048) {
          *(LAS v2u*)(kx + 2 * (pp - 4)) = v2u{vv, vv};
        }
      }
    } else {
      LAS unsigned char* g1 = tb + (16 + lr) * TROW;
      f32x4 c1 = {0, 0, 0, 0};
#pragma unroll
      for (int s = 0; s < 8; ++s) c1 = mfma_f16(*(LAS v4u*)(g1 + s * 64), *(LAS v4u*)(fb + s * 64), c1);
      c1 *= kscale;
      if (lq < 2) {
        const int base = 256 + 128 * lq + 8 * lr;
        const unsigned d0 = pk_bf16(c1[0], c1[0]), d1 = pk_bf16(c1[1], c1[2]), d2 = pk_bf16(c1[2], c1[2]), d3 = pk_bf16(c1[3], c1[3]);
        if (base != 256) *(LAS u16*)(kx + 2 * (base - 1)) = (u16)d0;
        *(LAS v2u*)(kx + 2 * base) = v2u{d0, d1};
        *(LAS unsigned*)(kx + 2 * (base + 4)) = d2;
        *(LAS u16*)(kx + 2 * (base + 6)) = (u16)d3;
      } else {
#pragma unroll
        for (int e = 0; e < 2; ++e) {
          const int base = 128 * (4 + 2 * (lq - 2) + e) + 8 * lr;
          const unsigned d0 = pk_bf16(c1[2 * e], c1[2 * e]), d1 = pk_bf16(c1[2 * e], c1[2 * e + 1]), d2 = pk_bf16(c1[2 * e + 1], c1[2 * e + 1]);
          if (base != 512) *(LAS unsigned*)(kx + 2 * (base - 2)) = d0;
          else *(LAS u16*)(kx + 2 * (base - 1)) = (u16)d0;
          *(LAS v2u*)(kx + 2 * base) = v2u{d0, d1};
          *(LAS unsigned*)(kx + 2 * (base + 4)) = d2;
        }
      }
    }
  }
  BAR();
  STAMP(4);
  {
    LAS unsigned char* kx = L + B_KX + ch * KX_CH + 32;
    LAS unsigned char* cp = L + ch * CPY_CH;
#pragma unroll
    for (int it = 0; it < 3; ++it) {
      const int a = (it < 2) ? n + 128 * it : 256 + (n & 1);
      if (it < 2 || n < 2) {
        const int L0 = 2048 - 8 * a;
        const v4u lo = *(LAS v4u*)(kx + 2 * (L0 - 8));
        const v2u hi = *(LAS v2u*)(kx + 2 * L0);
        const unsigned E0 = __builtin_amdgcn_alignbit(hi.y, hi.y, 16), E1 = __builtin_amdgcn_alignbit(hi.x, hi.x, 16);
        const unsigned E2 = __builtin_amdgcn_alignbit(lo.w, lo.w, 16), E3 = __builtin_amdgcn_alignbit(lo.z, lo.z, 16);
        const unsigned E4 = __builtin_amdgcn_alignbit(lo.y, lo.y, 16);
        const unsigned O0 = (hi.y & 0xFFFFu) | (hi.x & 0xFFFF0000u), O1 = (hi.x & 0xFFFFu) | (lo.w & 0xFFFF0000u);
        const unsigned O2 = (lo.w & 0xFFFFu) | (lo.z & 0xFFFF0000u), O3 = (lo.z & 0xFFFFu) | (lo.y & 0xFFFF0000u);
        const unsigned O4 = (lo.y & 0xFFFFu) | (lo.x & 0xFFFF0000u);
        *(LAS v4u*)(cp + 3 * CPY + 16 * a) = v4u{E0, E1, E2, E3};
        *(LAS v4u*)(cp + 2 * CPY + 16 * a) = v4u{O0, O1, O2, O3};
        *(LAS v4u*)(cp + 1 * CPY + 16 * a) = v4u{E1, E2, E3, E4};
        *(LAS v4u*)(cp + 0 * CPY + 16 * a) = v4u{O1, O2, O3, O4};
      }
    }
  }
  STAMP(5);
  wait_flags(p.ws, WS_FUT + (g * 32 + (lane & 31)) * FL, lane < 32, tag);
  STAMP(6);
  {
    const int quad = 16 * g + (m >> 1);
    const int off = WS_UT + (quad * 256 + (tid >> 1)) * 64 + ((m & 1) * 2 + (tid & 1)) * 16;
    const v4u a0 = ld_sc1(rs, off), a1 = ld_sc1(rs, off + 128 * 64);
    LAS unsigned char* q = L + B_U + (tid & 1) * U_CH + 512 + (tid >> 1) * 16;
    *(LAS v4u*)q = a0;
    *(LAS v4u*)(q + 2048) = a1;
  }
  BAR();
  STAMP(7);
  f32x4 acc[8];
#pragma unroll
  for (int i = 0; i < 8; ++i) acc[i] = f32x4{0, 0, 0, 0};
  {
    LAS unsigned char* ab = L + ch * CPY_CH + (lr & 3) * CPY + 2 * (2032 - 1792 - 224 + 8 * lq - 4 * (lr >> 2));
    LAS unsigned char* ub = L + B_U + ch * U_CH + 512 + 2 * (16 * lr - 16 + 8 * lq - 224);
#pragma unroll 1
    for (int rr = 0; rr < 4; ++rr) {
      const int rho = 32 * (2 * rr + (w & 1));
      LAS unsigned char* ap = ab + 2 * (224 - rho);
      LAS unsigned char* bp = ub + 2 * (224 - rho);
      v4u af[8], bf[8];
#pragma unroll
      for (int i = 0; i < 8; ++i) {
        const v2u x0 = *(LAS v2u*)(ap + 512 * (7 - i)), x1 = *(LAS v2u*)(ap + 512 * (7 - i) + 8);
        af[i] = v4u{x0.x, x0.y, x1.x, x1.y};
        bf[i] = *(LAS v4u*)(bp + 512 * i);
      }
#pragma unroll
      for (int i = 0; i < 8; ++i)
#pragma unroll
        for (int dl = 0; dl + i < 8; ++dl) acc[i + dl] = mfma_bf16(af[i], bf[dl], acc[i + dl]);
    }
  }
  STAMP(8);
  {
    LAS unsigned char* X = L + 36864 + w * 4096;
    const int keep = (w & 1) * 4;
    f32x4 mine[4];
#pragma unroll
    for (int i = 0; i < 4; ++i) {
      f32x4 lo = acc[i], hi = acc[4 + i];
      asm("" : "+v"(lo), "+v"(hi));
      *(LAS f32x4*)(X + i * 1024 + lane * 16) = (w & 1) ? lo : hi;
      mine[i] = (w & 1) ? hi : lo;
    }
    BAR();
    LAS unsigned char* Y = L + 36864 + (w ^ 1) * 4096;
    const float dk = bf2f(r_dsk);
    v2u yo[4];
#pragma unroll
    for (int i = 0; i < 4; ++i) {
      const f32x4 s = mine[i] + *(LAS f32x4*)(Y + i * 1024 + lane * 16);
      const v2u uu = *(LAS v2u*)(L + B_U + ch * U_CH + 512 + 2 * (256 * (keep + i) + 16 * lr + 4 * lq));
      const unsigned y01 = pk_bf16(s[0], s[1]), y23 = pk_bf16(s[2], s[3]);
      const unsigned k01 = pk_bf16(bf2f(uu.x & 0xFFFFu) * dk, __uint_as_float(uu.x & 0xFFFF0000u) * dk);
      const unsigned k23 = pk_bf16(bf2f(uu.y & 0xFFFFu) * dk, __uint_as_float(uu.y & 0xFFFF0000u) * dk);
      yo[i].x = pk_bf16(bf2f(y01 & 0xFFFFu) + bf2f(k01 & 0xFFFFu), __uint_as_float(y01 & 0xFFFF0000u) + __uint_as_float(k01 & 0xFFFF0000u));
      yo[i].y = pk_bf16(bf2f(y23 & 0xFFFFu) + bf2f(k23 & 0xFFFFu), __uint_as_float(y23 & 0xFFFF0000u) + __uint_as_float(k23 & 0xFFFF0000u));
    }
#pragma unroll
    for (int i = 0; i < 4; i += 2) {
      auto r0 = __builtin_amdgcn_permlane16_swap(yo[i].x, yo[i + 1].x, false, false);
      auto r1 = __builtin_amdgcn_permlane16_swap(yo[i].y, yo[i + 1].y, false, false);
      const int win = keep + i + (lq & 1);
      st_sc1(rs, WS_YT + ((d * 8 + win) * 2 + (lq >> 1)) * 256 + lr * 16, v4u{r0[0], r1[0], r0[1], r1[1]});
    }
    DRAIN();
  }
  BAR();
  if (tid == 0) set_flag(p.ws, WS_FY + (g * 32 + m) * FL, tag);
  STAMP(9);
  wait_flags(p.ws, WS_FY + (g * 32 + (lane & 31)) * FL, lane < 32, tag);
  STAMP(10);
  {
    const int c0 = tid >> 3, hh = (tid >> 2) & 1, l4 = tid & 3;
    const int off = WS_YT + (((64 * g + c0) * 8 + (m >> 2)) * 2 + hh) * 256 + (4 * (m & 3) + l4) * 16;
    const v4u a0 = ld_sc1(rs, off), a1 = ld_sc1(rs, off + 32 * 4096);
    if (m == 0 && tid == 0) set_flag(p.ws, WS_EPB + 256 * g, tag);
    LAS unsigned char* q = L + c0 * 128 + l4 * 32 + hh * 16;
    *(LAS v4u*)q = a0;
    *(LAS v4u*)(q + 4096) = a1;
  }
  BAR();
  {
    LAS unsigned char* q = L + (8 * lq + ((lane >> 2) & 3)) * 128 + 32 * w + 8 * (lane & 3);
    GAS unsigned char* op = (GAS unsigned char*)(p.out + (size_t)(64 * m + 16 * w + lr) * 1024 + 512 + 64 * g + 8 * lq);
#pragma unroll
    for (int it = 0; it < 2; ++it) *(GAS v4u*)(op + it * 64) = tr_read2(q + it * 4096, q + it * 4096 + 512);
  }
  STAMP(11);
}

__global__ void __launch_bounds__(256, 2) fused_kernel(Params p) {
  extern __shared__ __attribute__((aligned(16))) unsigned char smem[];
  LAS unsigned char* L = (LAS unsigned char*)smem;
  if (blockIdx.x < 256) roleA(p, L, blockIdx.x);
  else roleB(p, L, blockIdx.x - 256);
}

extern "C" void kernel_launch(void* const* d_in, const int* in_sizes, int n_in, void* d_out, int out_size, void* d_ws,
                              size_t ws_size, hipStream_t stream) {
  Params p;
  p.x = (const u16*)d_in[0];
  p.wqkv = (const u16*)d_in[1];
  p.wo = (const u16*)d_in[2];
  p.u = (const u16*)d_in[3];
  p.lar = (const u16*)d_in[4];
  p.aim = (const u16*)d_in[5];
  p.cre = (const u16*)d_in[6];
  p.cim = (const u16*)d_in[7];
  p.ldt = (const u16*)d_in[8];
  p.dsk = (const u16*)d_in[9];
  p.out = (u16*)d_out;
  p.ws = (unsigned char*)d_ws;
  (void)hipFuncSetAttribute((const void*)fused_kernel, hipFuncAttributeMaxDynamicSharedMemorySize, LDS_BYTES);
  fused_kernel<<<512, 256, LDS_BYTES, stream>>>(p);
}
```

```cpp
#include <hip/hip_runtime.h>
#include <stdint.h>

#define LAS __attribute__((address_space(3)))
#define GAS __attribute__((address_space(1)))
#define DEV __device__ __forceinline__
typedef unsigned short u16;
typedef unsigned long long u64;
typedef unsigned v4u __attribute__((ext_vector_type(4)));
typedef unsigned v2u __attribute__((ext_vector_type(2)));
typedef short s16x4 __attribute__((ext_vector_type(4)));
typedef __bf16 bf16x8 __attribute__((ext_vector_type(8)));
typedef __bf16 bf16x2 __attribute__((ext_vector_type(2)));
typedef _Float16 f16x2 __attribute__((ext_vector_type(2)));
typedef _Float16 f16x8 __attribute__((ext_vector_type(8)));
typedef float f32x4 __attribute__((ext_vector_type(4)));
typedef float f32x2 __attribute__((ext_vector_type(2)));
typedef GAS u64 gu64;

#define STAMPS 0

constexpr int FL = 128;
constexpr int WS_EPA = 0;
constexpr int WS_EPB = 256;
constexpr int WS_FKV = 4096;
constexpr int WS_FUT = WS_FKV + 256 * FL;
constexpr int WS_FY = WS_FUT + 256 * FL;
constexpr int WS_FO = WS_FY + 256 * FL;
constexpr int KVP_REC = 2304;
constexpr int WS_KVP = WS_FO + 1024 * FL;
constexpr int WS_O = WS_KVP + 256 * KVP_REC;
constexpr int WS_UT = WS_O + 2 * 1024 * 1024;
constexpr int WS_YT = WS_UT + 2 * 1024 * 1024;
constexpr int WS_STAMP = WS_YT + 2 * 1024 * 1024;
constexpr int WS_BYTES = WS_STAMP + 512 * 4 * 16 * 8;
constexpr int LDS_BYTES = 80 * 1024;
constexpr unsigned SPIN_CAP = 1u << 18;

struct Params {
  const u16 *x, *wqkv, *wo, *u, *lar, *aim, *cre, *cim, *ldt, *dsk;
  u16* out;
  unsigned char* ws;
};

DEV float bf2f(unsigned v) { return __uint_as_float(v << 16); }
DEV unsigned pk_bf16(float a, float b) {
  f32x2 v = {a, b};
  return __builtin_bit_cast(unsigned, __builtin_convertvector(v, bf16x2));
}
DEV unsigned pk_f16(float a, float b) {
  f32x2 v = {a, b};
  return __builtin_bit_cast(unsigned, __builtin_convertvector(v, f16x2));
}
DEV float rbf(float a) { return __uint_as_float(pk_bf16(a, a) << 16); }
DEV f32x4 mfma_bf16(v4u a, v4u b, f32x4 c) {
  return __builtin_amdgcn_mfma_f32_16x16x32_bf16(__builtin_bit_cast(bf16x8, a), __builtin_bit_cast(bf16x8, b), c, 0, 0, 0);
}
DEV f32x4 mfma_f16(v4u a, v4u b, f32x4 c) {
  return __builtin_amdgcn_mfma_f32_16x16x32_f16(__builtin_bit_cast(f16x8, a), __builtin_bit_cast(f16x8, b), c, 0, 0, 0);
}
DEV f32x4 mfma16_bf16(v2u a, v2u b, f32x4 c) {
  return __builtin_amdgcn_mfma_f32_16x16x16bf16_1k(__builtin_bit_cast(s16x4, a), __builtin_bit_cast(s16x4, b), c, 0, 0, 0);
}
DEV v2u tr_read(LAS unsigned char* p) {
  return __builtin_bit_cast(v2u, __builtin_amdgcn_ds_read_tr16_b64_v4i16((LAS s16x4*)p));
}
DEV v4u tr_read2(LAS unsigned char* p0, LAS unsigned char* p1) {
  v2u a = tr_read(p0), b = tr_read(p1);
  return v4u{a.x, a.y, b.x, b.y};
}
DEV void glds16(const void* sbase, unsigned voff, unsigned lds_dst) {
  unsigned keep;
  asm volatile("s_mov_b32 %0, m0\n\ts_mov_b32 m0, %3\n\ts_nop 0\n\tglobal_load_lds_dwordx4 %1, %2\n\ts_mov_b32 m0, %0"
               : "=&s"(keep) : "v"(voff), "s"(sbase), "s"(lds_dst) : "memory");
}
#define DRAIN() asm volatile("s_waitcnt vmcnt(0)" ::: "memory")
#define BAR() do { asm volatile("s_waitcnt lgkmcnt(0)" ::: "memory"); __builtin_amdgcn_s_barrier(); asm volatile("" ::: "memory"); } while (0)
#define RLX_AGENT __ATOMIC_RELAXED, __HIP_MEMORY_SCOPE_AGENT
constexpr int AUX_SC1 = 16;
typedef __amdgpu_buffer_rsrc_t rsrc_t;
DEV v4u ld_sc1(rsrc_t rs, int off) { return __builtin_amdgcn_raw_buffer_load_b128(rs, off, 0, AUX_SC1); }
DEV void st_sc1(rsrc_t rs, int off, v4u v) { __builtin_amdgcn_raw_buffer_store_b128(v, rs, off, 0, AUX_SC1); }
DEV void wait_flags(unsigned char* ws, int off, bool active, u64 tag) {
  gu64* f = (gu64*)(ws + off);
#pragma unroll 1
  for (unsigned spins = 0; spins < SPIN_CAP; ++spins) {
    bool ok = true;
    if (active) ok = __hip_atomic_load(f, RLX_AGENT) == tag;
    if (__all(ok)) break;
    __builtin_amdgcn_s_sleep(1);
  }
  __builtin_amdgcn_fence(__ATOMIC_ACQUIRE, "wavefront");
}
DEV void set_flag(unsigned char* ws, int off, u64 tag) { __hip_atomic_store((gu64*)(ws + off), tag, RLX_AGENT); }

#define STAMP_DECL
#define STAMP(k) do { } while (0)
#define STAMP_END(n) do { } while (0)

DEV float phi(float t) {
  float e = __builtin_amdgcn_exp2f(t * 1.44269504088896341f);
  e = __builtin_amdgcn_fmed3f(e, 0.f, 1.f);
  return (fmaxf(t, 0.f) + e) + 1e-6f;
}
template <int V> struct IC { static constexpr int value = V; };
struct cf { float x, y; };
DEV cf cmul(cf a, cf b) {
  cf r;
  r.x = __builtin_fmaf(-a.y, b.y, a.x * b.x);
  r.y = __builtin_fmaf(a.y, b.x, a.x * b.y);
  asm("" : "+v"(r.x));
  return r;
}
DEV void gld16(v4u& dst, const void* sbase, unsigned voff) {
  asm volatile("global_load_dwordx4 %0, %1, %2" : "=v"(dst) : "v"(voff), "s"(sbase) : "memory");
}
DEV void gld16_sc1(v4u& dst, const void* sbase, unsigned voff) {
  asm volatile("global_load_dwordx4 %0, %1, %2 sc1" : "=v"(dst) : "v"(voff), "s"(sbase) : "memory");
}
template <int N> DEV void vmwait4(v4u (&r)[4]) {
  asm volatile("s_waitcnt vmcnt(%4)" : "+v"(r[0]), "+v"(r[1]), "+v"(r[2]), "+v"(r[3]) : "n"(N) : "memory");
}
template <int N> DEV void vmwait() { asm volatile("s_waitcnt vmcnt(%0)" ::"n"(N) : "memory"); }
#define SB0() __builtin_amdgcn_sched_barrier(0)
#define SGB(mask, n) __builtin_amdgcn_sched_group_barrier(mask, n, 0)

DEV void roleA(const Params& p, LAS unsigned char* L, const int a) {
  const int tid = threadIdx.x, lane = tid & 63, w = __builtin_amdgcn_readfirstlane(tid >> 6);
  const int lr = lane & 15, lq = lane >> 4;
  const int g = a & 7, m = a >> 3;
  const int bi = 4 * (g >> 1) + (m >> 3), bj = 8 * (g & 1) + (m & 7);
  const unsigned Lb = (unsigned)(uintptr_t)L;
  const rsrc_t rs = __builtin_amdgcn_make_buffer_rsrc(p.ws, 0, WS_BYTES, 0x00020000);
  STAMP_DECL
  STAMP(0);
  const int rot = ((m & 7) + 2 * (m >> 3)) & 7;
  const unsigned wvoff = (unsigned)(((16 * w + (lane >> 2)) * 1536 + 32 * bj + 8 * ((lane & 3) ^ ((lane >> 5) << 1))) * 2);
  const unsigned xvoff = (unsigned)((128 * bi + 32 * w + (lane >> 3)) * 1024 + (((lane & 7) ^ (lane >> 3)) * 16));
  auto issue = [&](int c) {
    const int kc = ((c + rot) & 7) * 64;
#pragma unroll
    for (int sg = 0; sg < 2; ++sg)
      glds16(p.wqkv + (size_t)kc * 1536 + 512 * (1 + sg), wvoff, Lb + 49152 + (c % 3) * 8192 + sg * 4096 + w * 1024);
#pragma unroll
    for (int r = 0; r < 4; ++r) glds16(p.x + 8 * r * 512 + kc, xvoff, Lb + (c % 3) * 16384 + w * 4096 + r * 1024);
  };
  auto issue_q = [&](int h) {
#pragma unroll
    for (int c = 0; c < 4; ++c)
      glds16(p.wqkv + (size_t)(((4 * h + c + rot) & 7) * 64) * 1536, wvoff, Lb + ((2 + h) % 3) * 16384 + c * 4096 + w * 1024);
  };
  const u64 tag = __hip_atomic_load((gu64*)(p.ws + WS_EPA), RLX_AGENT) + 1;
  issue(0);
  issue(1);
  SB0();
  STAMP(1);
  LAS unsigned char* tb0 = L + (8 * lq + ((lane >> 2) & 3)) * 64 + 32 * (lq & 1) + 8 * (lane & 3);
  LAS unsigned char* tb1 = L + (8 * lq + ((lane >> 2) & 3)) * 64 + 32 * ((lq & 1) ^ 1) + 8 * (lane & 3);
  LAS unsigned char* xr0 = L + w * 4096 + lr * 128 + ((lq ^ (lr & 7)) * 16);
  LAS unsigned char* xr1 = L + w * 4096 + lr * 128 + (((4 + lq) ^ (lr & 7)) * 16);
  v4u xf[8][4];
  f32x4 ak[2][2], av[2][2];
#pragma unroll
  for (int r = 0; r < 2; ++r)
#pragma unroll
    for (int h = 0; h < 2; ++h) { ak[r][h] = f32x4{0, 0, 0, 0}; av[r][h] = f32x4{0, 0, 0, 0}; }
  auto kv_chunk = [&](auto cc) {
    constexpr int c = decltype(cc)::value;
    constexpr int wo_ = 49152 + (c % 3) * 8192, xo_ = (c % 3) * 16384;
    vmwait<(c < 7) ? 6 : 4>();
    __builtin_amdgcn_s_barrier();
    asm volatile("" ::: "memory");
    v4u wa[4], wb[4];
    xf[c][0] = *(LAS v4u*)(xr0 + xo_);
    wa[0] = tr_read2(tb0 + wo_, tb0 + wo_ + 256);
    wa[1] = tr_read2(tb1 + wo_, tb1 + wo_ + 256);
    xf[c][1] = *(LAS v4u*)(xr0 + xo_ + 2048);
    wa[2] = tr_read2(tb0 + wo_ + 4096, tb0 + wo_ + 4096 + 256);
    wa[3] = tr_read2(tb1 + wo_ + 4096, tb1 + wo_ + 4096 + 256);
    SB0();
    if constexpr (c + 2 < 8) issue(c + 2); else issue_q(c - 6);
    SB0();
    xf[c][2] = *(LAS v4u*)(xr1 + xo_);
    wb[0] = tr_read2(tb0 + wo_ + 2048, tb0 + wo_ + 2048 + 256);
    wb[1] = tr_read2(tb1 + wo_ + 2048, tb1 + wo_ + 2048 + 256);
    xf[c][3] = *(LAS v4u*)(xr1 + xo_ + 2048);
    wb[2] = tr_read2(tb0 + wo_ + 4096 + 2048, tb0 + wo_ + 4096 + 2048 + 256);
    wb[3] = tr_read2(tb1 + wo_ + 4096 + 2048, tb1 + wo_ + 4096 + 2048 + 256);
#pragma unroll
    for (int r = 0; r < 2; ++r) {
      ak[r][0] = mfma_bf16(xf[c][r], wa[0], ak[r][0]);
      ak[r][1] = mfma_bf16(xf[c][r], wa[1], ak[r][1]);
      av[r][0] = mfma_bf16(xf[c][r], wa[2], av[r][0]);
      av[r][1] = mfma_bf16(xf[c][r], wa[3], av[r][1]);
    }
#pragma unroll
    for (int i = 0; i < 5; ++i) { SGB(0x8, 1); SGB(0x100, 2); }
    SB0();
#pragma unroll
    for (int r = 0; r < 2; ++r) {
      ak[r][0] = mfma_bf16(xf[c][2 + r], wb[0], ak[r][0]);
      ak[r][1] = mfma_bf16(xf[c][2 + r], wb[1], ak[r][1]);
      av[r][0] = mfma_bf16(xf[c][2 + r], wb[2], av[r][0]);
      av[r][1] = mfma_bf16(xf[c][2 + r], wb[3], av[r][1]);
    }
    SB0();
  };
  kv_chunk(IC<0>{}); kv_chunk(IC<1>{}); kv_chunk(IC<2>{}); kv_chunk(IC<3>{});
  kv_chunk(IC<4>{}); kv_chunk(IC<5>{}); kv_chunk(IC<6>{}); kv_chunk(IC<7>{});
  STAMP(2);
  LAS unsigned char* S = L + 73728;
  LAS unsigned char* SZ = L + 49152;
  {
    const v4u ones = {0x3F803F80u, 0x3F803F80u, 0x3F803F80u, 0x3F803F80u};
#pragma unroll
    for (int h = 0; h < 2; ++h) {
      v4u pk, vv;
      pk.x = pk_bf16(phi(ak[0][h][0]), phi(ak[0][h][1]));
      pk.y = pk_bf16(phi(ak[0][h][2]), phi(ak[0][h][3]));
      pk.z = pk_bf16(phi(ak[1][h][0]), phi(ak[1][h][1]));
      pk.w = pk_bf16(phi(ak[1][h][2]), phi(ak[1][h][3]));
      vv.x = pk_bf16(av[0][h][0], av[0][h][1]);
      vv.y = pk_bf16(av[0][h][2], av[0][h][3]);
      vv.z = pk_bf16(av[1][h][0], av[1][h][1]);
      vv.w = pk_bf16(av[1][h][2], av[1][h][3]);
      const f32x4 kv = mfma_bf16(pk, vv, f32x4{0, 0, 0, 0});
      const f32x4 zz = mfma_bf16(pk, ones, f32x4{0, 0, 0, 0});
      *(LAS f32x4*)(S + w * 2048 + h * 1024 + lane * 16) = kv;
      if (lr == 0) *(LAS f32x4*)(SZ + w * 128 + h * 64 + lq * 16) = zz;
    }
  }
  asm volatile("" ::"v"(tag));
  DRAIN();
  BAR();
  if (w == 0) {
    const int rec = WS_KVP + (bj * 16 + bi) * KVP_REC;
    f32x4 s0 = *(LAS f32x4*)(S + lane * 16), s1 = *(LAS f32x4*)(S + 1024 + lane * 16);
    f32x4 sz = *(LAS f32x4*)(SZ + (lane & 7) * 16);
#pragma unroll
    for (int ww = 1; ww < 4; ++ww) {
      s0 += *(LAS f32x4*)(S + ww * 2048 + lane * 16);
      s1 += *(LAS f32x4*)(S + ww * 2048 + 1024 + lane * 16);
      sz += *(LAS f32x4*)(SZ + ww * 128 + (lane & 7) * 16);
    }
    st_sc1(rs, rec + lane * 16, __builtin_bit_cast(v4u, s0));
    st_sc1(rs, rec + 1024 + lane * 16, __builtin_bit_cast(v4u, s1));
    if (lane < 8) st_sc1(rs, rec + 2048 + lane * 16, __builtin_bit_cast(v4u, sz));
    DRAIN();
    if (lane == 0) set_flag(p.ws, WS_FKV + (bj * 16 + bi) * FL, tag);
  }
  STAMP(3);
  const int R = 4 * g + (m >> 3), C = m & 7;
  const unsigned ovoff = (unsigned)(((lane >> 3) * 512 + 64 * C + 8 * ((lane & 7) ^ (((lane >> 4) & 3) << 1))) * 2);
#pragma unroll
  for (int n = 0; n < 8; ++n) glds16(p.wo + (size_t)(64 * w + 8 * n) * 512, ovoff, Lb + ((w < 2) ? 16384 + w * 8192 : 57344 + (w - 2) * 8192) + n * 1024);
  f32x4 aq[2][2];
#pragma unroll
  for (int r = 0; r < 2; ++r)
#pragma unroll
    for (int h = 0; h < 2; ++h) aq[r][h] = f32x4{0, 0, 0, 0};
  {
    v4u qa[4], qb[4];
    auto ldq = [&](v4u (&q)[4], int c) {
      const int o = (c < 4) ? 32768 + c * 4096 : (c - 4) * 4096;
      q[0] = tr_read2(tb0 + o, tb0 + o + 256);
      q[1] = tr_read2(tb1 + o, tb1 + o + 256);
      q[2] = tr_read2(tb0 + o + 2048, tb0 + o + 2048 + 256);
      q[3] = tr_read2(tb1 + o + 2048, tb1 + o + 2048 + 256);
    };
    auto mmq = [&](v4u (&q)[4], int c) {
#pragma unroll
      for (int kk = 0; kk < 2; ++kk)
#pragma unroll
        for (int r = 0; r < 2; ++r) {
          aq[r][0] = mfma_bf16(q[2 * kk], xf[c][2 * kk + r], aq[r][0]);
          aq[r][1] = mfma_bf16(q[2 * kk + 1], xf[c][2 * kk + r], aq[r][1]);
        }
    };
    ldq(qa, 0);
    SB0();
#pragma unroll
    for (int c = 0; c < 8; c += 2) {
      ldq(qb, c + 1);
      mmq(qa, c);
#pragma unroll
      for (int i = 0; i < 8; ++i) { SGB(0x8, 1); SGB(0x100, 1); }
      SB0();
      if (c + 2 < 8) ldq(qa, c + 2);
      mmq(qb, c + 1);
#pragma unroll
      for (int i = 0; i < 8; ++i) { SGB(0x8, 1); SGB(0x100, 1); }
      SB0();
    }
  }
  v2u pq[2][2];
#pragma unroll
  for (int r = 0; r < 2; ++r)
#pragma unroll
    for (int h = 0; h < 2; ++h) {
      pq[r][h].x = pk_bf16(phi(aq[r][h][0]), phi(aq[r][h][1]));
      pq[r][h].y = pk_bf16(phi(aq[r][h][2]), phi(aq[r][h][3]));
    }
  BAR();
  STAMP(4);
  wait_flags(p.ws, WS_FKV + (bj * 16 + 4 * w + (lane & 3)) * FL, lane < 4, tag);
  STAMP(5);
  {
    v4u t0[4], t1[4], tz[4];
#pragma unroll
    for (int q = 0; q < 4; ++q) {
      const int rec = WS_KVP + (bj * 16 + 4 * w + q) * KVP_REC;
      gld16_sc1(t0[q], p.ws + rec, lane * 16);
      gld16_sc1(t1[q], p.ws + rec + 1024, lane * 16);
      gld16_sc1(tz[q], p.ws + rec + 2048, (lane & 7) * 16);
    }
    SB0();
#pragma unroll
    for (int n = 0; n < 8; ++n) glds16(p.wo + (size_t)(256 + 64 * w + 8 * n) * 512, ovoff, Lb + ((w < 2) ? w * 8192 : 32768 + (w - 2) * 8192) + n * 1024);
    asm volatile("s_waitcnt vmcnt(8)" : "+v"(t0[0]), "+v"(t0[1]), "+v"(t0[2]), "+v"(t0[3]), "+v"(t1[0]), "+v"(t1[1]), "+v"(t1[2]), "+v"(t1[3]), "+v"(tz[0]), "+v"(tz[1]), "+v"(tz[2]), "+v"(tz[3]));
    f32x4 s0 = __builtin_bit_cast(f32x4, t0[0]), s1 = __builtin_bit_cast(f32x4, t1[0]), sz = __builtin_bit_cast(f32x4, tz[0]);
#pragma unroll
    for (int q = 1; q < 4; ++q) {
      s0 += __builtin_bit_cast(f32x4, t0[q]);
      s1 += __builtin_bit_cast(f32x4, t1[q]);
      sz += __builtin_bit_cast(f32x4, tz[q]);
    }
    *(LAS f32x4*)(S + w * 2048 + lane * 16) = s0;
    *(LAS f32x4*)(S + w * 2048 + 1024 + lane * 16) = s1;
    if (lane < 8) *(LAS f32x4*)(SZ + w * 128 + lane * 16) = sz;
  }
  BAR();
  STAMP(6);
  {
    v2u kvb[2], zb[2];
#pragma unroll
    for (int h = 0; h < 2; ++h) {
      f32x4 s = *(LAS f32x4*)(S + h * 1024 + lane * 16);
      f32x4 z = *(LAS f32x4*)(SZ + h * 64 + lq * 16);
#pragma unroll
      for (int ww = 1; ww < 4; ++ww) {
        s += *(LAS f32x4*)(S + ww * 2048 + h * 1024 + lane * 16);
        z += *(LAS f32x4*)(SZ + ww * 128 + h * 64 + lq * 16);
      }
      kvb[h] = v2u{pk_bf16(s[0], s[1]), pk_bf16(s[2], s[3])};
      zb[h] = v2u{pk_bf16(z[0], z[1]), pk_bf16(z[2], z[3])};
    }
#pragma unroll
    for (int r = 0; r < 2; ++r) {
      v4u ov;
#pragma unroll
      for (int h = 0; h < 2; ++h) {
        const f32x4 num = mfma16_bf16(kvb[h], pq[r][h], f32x4{0, 0, 0, 0});
        const f32x4 den = mfma16_bf16(zb[h], pq[r][h], f32x4{0, 0, 0, 0});
        const float rd = __builtin_amdgcn_rcpf(den[0] + 1e-6f);
        const unsigned lo = pk_bf16(num[0] * rd, num[1] * rd), hi = pk_bf16(num[2] * rd, num[3] * rd);
        if (h == 0) { ov.x = lo; ov.y = hi; } else { ov.z = lo; ov.w = hi; }
      }
      st_sc1(rs, WS_O + ((8 * bi + 2 * w + r) * 16 + bj) * 1024 + lane * 16, ov);
    }
    DRAIN();
    if (lane == 0) set_flag(p.ws, WS_FO + ((bi * 4 + w) * 16 + bj) * FL, tag);
  }
  STAMP(7);
  const int T = 4 * R + w;
  wait_flags(p.ws, WS_FO + ((T >> 1) * 16 + lr) * FL, lane < 16, tag);
  STAMP(8);
  v4u of[16];
#pragma unroll
  for (int j = 0; j < 16; ++j) of[j] = ld_sc1(rs, WS_O + (T * 16 + j) * 1024 + lane * 16);
  if (a == 0 && tid == 0) set_flag(p.ws, WS_EPA, tag);
  __builtin_amdgcn_sched_barrier(0);
  asm volatile("" ::"v"(of[0]));
  BAR();
  STAMP(9);
  f32x4 acc[4];
#pragma unroll
  for (int c = 0; c < 4; ++c) acc[c] = f32x4{0, 0, 0, 0};
  {
    const int ra = 4 * lq + ((lane >> 2) & 3);
    const int sw = (ra >> 1) & 3;
    LAS unsigned char* ob = L + 128 * ra + 8 * (lane & 3);
    v4u fa[4], fb[4];
    auto ldw = [&](v4u (&f)[4], int j) {
      const int off = (j < 4) ? 16384 + j * 4096 : (j < 8) ? 57344 + (j - 4) * 4096 : (j < 12) ? (j - 8) * 4096 : 32768 + (j - 12) * 4096;
#pragma unroll
      for (int c = 0; c < 4; ++c) {
        LAS unsigned char* q = ob + 32 * (c ^ sw) + off;
        f[c] = tr_read2(q, q + 2048);
      }
    };
    ldw(fa, 0);
    SB0();
#pragma unroll
    for (int j = 0; j < 16; j += 2) {
      ldw(fb, j + 1);
#pragma unroll
      for (int c = 0; c < 4; ++c) acc[c] = mfma_bf16(fa[c], of[j], acc[c]);
#pragma unroll
      for (int i = 0; i < 4; ++i) { SGB(0x8, 1); SGB(0x100, 2); }
      SB0();
      if (j + 2 < 16) ldw(fa, j + 2);
#pragma unroll
      for (int c = 0; c < 4; ++c) acc[c] = mfma_bf16(fb[c], of[j + 1], acc[c]);
#pragma unroll
      for (int i = 0; i < 4; ++i) { SGB(0x8, 1); SGB(0x100, 2); }
      SB0();
    }
  }
  STAMP(10);
  {
    GAS unsigned char* op = (GAS unsigned char*)(p.out + (size_t)(16 * T + lr) * 1024 + 64 * C) + (lq & 1) * 32 + (lq >> 1) * 16;
#pragma unroll
    for (int c = 0; c < 4; c += 2) {
      unsigned a0 = pk_bf16(acc[c][0], acc[c][1]), a1 = pk_bf16(acc[c][2], acc[c][3]);
      unsigned b0 = pk_bf16(acc[c + 1][0], acc[c + 1][1]), b1 = pk_bf16(acc[c + 1][2], acc[c + 1][3]);
      auto r0 = __builtin_amdgcn_permlane16_swap(a0, b0, false, false);
      auto r1 = __builtin_amdgcn_permlane16_swap(a1, b1, false, false);
      *(GAS v4u*)(op + c * 32) = v4u{r0[0], r1[0], r0[1], r1[1]};
    }
  }
  STAMP(11);
  STAMP_END(12);
}

constexpr int TROW = 528;
constexpr int TAB_CH = 57 * TROW;
constexpr int B_TAB = 0;
constexpr int B_KX = 60416;
constexpr int KX_CH = 4352;
constexpr int B_U = B_KX + 2 * KX_CH;
constexpr int U_CH = 4608;
constexpr int CPY = 4160;
constexpr int CPY_CH = 4 * CPY;
static_assert(B_U + 2 * U_CH <= LDS_BYTES, "lds");

DEV cf cpow_direct(float al, float be, float pw) {
  const float mag = __builtin_amdgcn_exp2f(al * pw);
  const float ph = __builtin_amdgcn_fractf(be * pw);
  return cf{mag * __builtin_amdgcn_cosf(ph), mag * __builtin_amdgcn_sinf(ph)};
}

DEV void roleB(const Params& p, LAS unsigned char* L, const int b) {
  const int tid = threadIdx.x, lane = tid & 63, w = __builtin_amdgcn_readfirstlane(tid >> 6);
  const int lr = lane & 15, lq = lane >> 4;
  const int g = b & 7, m = b >> 3;
  const int ch = w >> 1;
  const int d = 64 * g + 2 * m + ch;
  const rsrc_t rs = __builtin_amdgcn_make_buffer_rsrc(p.ws, 0, WS_BYTES, 0x00020000);
  STAMP_DECL
  STAMP(0);
  const GAS v4u* up = (const GAS v4u*)(p.u + (size_t)(64 * m + (tid >> 3)) * 512 + 64 * g + 8 * (tid & 7));
  const v4u ut0 = up[0], ut1 = up[32 * 64];
  const u64 tag = __hip_atomic_load((gu64*)(p.ws + WS_EPB + 256 * g), RLX_AGENT) + 1;
  const int n = tid & 127;
  const unsigned r_lar = p.lar[d * 128 + n], r_aim = p.aim[d * 128 + n];
  const unsigned r_cre = p.cre[d * 128 + n], r_cim = p.cim[d * 128 + n];
  const unsigned r_cre2 = p.cre[d * 128 + (n ^ 64)], r_cim2 = p.cim[d * 128 + (n ^ 64)];
  const unsigned r_ldt = p.ldt[d], r_dsk = p.dsk[d];
  {
    unsigned t;
    if (w == 0) t = *(const GAS unsigned*)(p.wqkv + (size_t)(16 * m + (lane >> 2)) * 1536 + 256 * (g & 1) + 64 * (lane & 3));
    else t = *(const GAS unsigned*)(p.wo + (size_t)(m * 128 + ((w - 1) & 1) * 64 + lane) * 64);
    asm volatile("" ::"v"(t));
  }
  *(LAS v4u*)(L + (tid >> 3) * 128 + (tid & 7) * 16) = ut0;
  *(LAS v4u*)(L + 4096 + (tid >> 3) * 128 + (tid & 7) * 16) = ut1;
  if (tid < 64) *(LAS v4u*)(L + B_U + (tid >> 5) * U_CH + (tid & 31) * 16) = v4u{0, 0, 0, 0};
  else if (tid < 68) *(LAS v4u*)(L + B_KX + ((tid >> 1) & 1) * KX_CH + (tid & 1) * 16) = v4u{0, 0, 0, 0};
  BAR();
  STAMP(1);
  {
    LAS unsigned char* q = L + (8 * lq + ((lane >> 2) & 3)) * 128 + 32 * w + 8 * (lane & 3);
#pragma unroll
    for (int it = 0; it < 2; ++it) {
      const v4u v = tr_read2(q + it * 4096, q + it * 4096 + 512);
      const int quad = 16 * g + 4 * w + (lr >> 2), oct = 8 * m + 4 * it + lq;
      st_sc1(rs, WS_UT + (quad * 256 + oct) * 64 + (lr & 3) * 16, v);
    }
    DRAIN();
  }
  BAR();
  if (tid == 0) set_flag(p.ws, WS_FUT + (g * 32 + m) * FL, tag);
  STAMP(2);
  float kscale;
  {
    const float dt = rbf(expf(bf2f(r_ldt)));
    const float ar = -rbf(expf(bf2f(r_lar))) * dt;
    const float al = ar * 1.44269504088896341f;
    const float be = (bf2f(r_aim) * dt) * 0.15915494309189535f;
    unsigned mx = max(max(r_cre & 0x7FFFu, r_cim & 0x7FFFu), max(r_cre2 & 0x7FFFu, r_cim2 & 0x7FFFu));
#pragma unroll
    for (int o = 32; o >= 1; o >>= 1) mx = max(mx, (unsigned)__shfl_xor((int)mx, o));
    int ex = (int)(mx >> 7);
    ex = min(max(ex, 1), 253);
    const float cs = __uint_as_float((unsigned)(254 - ex) << 23);
    kscale = 2.f * dt * __uint_as_float((unsigned)ex << 23);
    const cf z1 = cpow_direct(al, be, 1.f), z8 = cpow_direct(al, be, 8.f), z128 = cpow_direct(al, be, 128.f);
    const cf z1024 = cpow_direct(al, be, 1024.f);
    LAS unsigned char* tp = L + B_TAB + ch * TAB_CH + n * 4;
#define PUTG(row, v) do { const cf v_ = (v); *(LAS unsigned*)(tp + (row) * TROW) = pk_f16(v_.x, v_.y); } while (0)
    cf zp[8];
    zp[0] = cf{1.f, 0.f};
    zp[1] = z1;
#pragma unroll
    for (int i = 2; i < 8; ++i) zp[i] = cmul(zp[i - 1], z1);
#pragma unroll
    for (int i = 0; i < 8; ++i) PUTG(i, zp[i]);
    cf f = cf{bf2f(r_cre) * cs, bf2f(r_cim) * cs};
    cf h = z128, h8 = z1024;
#pragma unroll
    for (int i = 0; i < 16; ++i) {
      *(LAS unsigned*)(tp + (41 + i) * TROW) = pk_f16(f.x, -f.y);
      if (i < 15) f = cmul(f, z8);
      if (i < 9) {
        PUTG(32 + i, h8);
        if (i < 8) h8 = cmul(h8, z128);
      }
      if (i >= 1 && i <= 7) {
        if (i == 1) {
          PUTG(8, h);
#pragma unroll
          for (int q = 1; q < 8; ++q) PUTG(8 + q, cmul(h, zp[q]));
        } else if (i <= 3) {
          PUTG(16 + 4 * (i - 2), h);
#pragma unroll
          for (int q = 1; q < 4; ++q) PUTG(16 + 4 * (i - 2) + q, cmul(h, zp[2 * q]));
        } else {
          PUTG(24 + 2 * (i - 4), h);
          PUTG(24 + 2 * (i - 4) + 1, cmul(h, zp[4]));
        }
        if (i < 7) h = cmul(h, z128);
      }
    }
#undef PUTG
  }
  BAR();
  STAMP(3);
  {
    LAS unsigned char* tb = L + B_TAB + ch * TAB_CH + lq * 16;
    LAS unsigned char* kx = L + B_KX + ch * KX_CH + 32;
    LAS unsigned char* fb = tb + (41 + lr) * TROW;
    if ((w & 1) == 0) {
      LAS unsigned char* g0 = tb + lr * TROW;
      LAS unsigned char* g2 = tb + (32 + min(lr, 8)) * TROW;
      f32x4 c0 = {0, 0, 0, 0}, c2 = {0, 0, 0, 0};
#pragma unroll
      for (int s = 0; s < 8; ++s) {
        const v4u fv = *(LAS v4u*)(fb + s * 64);
        c0 = mfma_f16(*(LAS v4u*)(g0 + s * 64), fv, c0);
        c2 = mfma_f16(*(LAS v4u*)(g2 + s * 64), fv, c2);
      }
      c0 *= kscale;
      c2 *= kscale;
      *(LAS v2u*)(kx + 2 * (128 * (lq >> 1) + 8 * lr + 4 * (lq & 1))) = v2u{pk_bf16(c0[0], c0[1]), pk_bf16(c0[2], c0[3])};
#pragma unroll
      for (int r = 0; r < 4; ++r) {
        const unsigned vv = pk_bf16(c2[r], c2[r]);
        const unsigned pv = (unsigned)__builtin_amdgcn_update_dpp(0, (int)vv, 0x111, 0xf, 0xf, false);
        const unsigned first = (lr & 1) ? ((pv & 0xFFFFu) | (vv & 0xFFFF0000u)) : vv;
        const int pp = 128 * (8 + 4 * lq + r) + 8 * lr;
        if (lq < 2) {
          if (pp != 1024) *(LAS unsigned*)(kx + 2 * (pp - 4)) = first;
          *(LAS unsigned*)(kx + 2 * (pp - 2)) = vv;
          *(LAS v2u*)(kx + 2 * pp) = v2u{vv, vv};
        } else if (pp == 2048) {
          *(LAS v2u*)(kx + 2 * (pp - 4)) = v2u{vv, vv};
        }
      }
    } else {
      LAS unsigned char* g1 = tb + (16 + lr) * TROW;
      f32x4 c1 = {0, 0, 0, 0};
#pragma unroll
      for (int s = 0; s < 8; ++s) c1 = mfma_f16(*(LAS v4u*)(g1 + s * 64), *(LAS v4u*)(fb + s * 64), c1);
      c1 *= kscale;
      if (lq < 2) {
        const int base = 256 + 128 * lq + 8 * lr;
        const unsigned d0 = pk_bf16(c1[0], c1[0]), d1 = pk_bf16(c1[1], c1[2]), d2 = pk_bf16(c1[2], c1[2]), d3 = pk_bf16(c1[3], c1[3]);
        if (base != 256) *(LAS u16*)(kx + 2 * (base - 1)) = (u16)d0;
        *(LAS v2u*)(kx + 2 * base) = v2u{d0, d1};
        *(LAS unsigned*)(kx + 2 * (base + 4)) = d2;
        *(LAS u16*)(kx + 2 * (base + 6)) = (u16)d3;
      } else {
#pragma unroll
        for (int e = 0; e < 2; ++e) {
          const int base = 128 * (4 + 2 * (lq - 2) + e) + 8 * lr;
          const unsigned d0 = pk_bf16(c1[2 * e], c1[2 * e]), d1 = pk_bf16(c1[2 * e], c1[2 * e + 1]), d2 = pk_bf16(c1[2 * e + 1], c1[2 * e + 1]);
          if (base != 512) *(LAS unsigned*)(kx + 2 * (base - 2)) = d0;
          else *(LAS u16*)(kx + 2 * (base - 1)) = (u16)d0;
          *(LAS v2u*)(kx + 2 * base) = v2u{d0, d1};
          *(LAS unsigned*)(kx + 2 * (base + 4)) = d2;
        }
      }
    }
  }
  BAR();
  STAMP(4);
  {
    LAS unsigned char* kx = L + B_KX + ch * KX_CH + 32;
    LAS unsigned char* cp = L + ch * CPY_CH;
#pragma unroll
    for (int it = 0; it < 3; ++it) {
      const int a = (it < 2) ? n + 128 * it : 256 + (n & 1);
      if (it < 2 || n < 2) {
        const int L0 = 2048 - 8 * a;
        const v4u lo = *(LAS v4u*)(kx + 2 * (L0 - 8));
        const v2u hi = *(LAS v2u*)(kx + 2 * L0);
        const unsigned E0 = __builtin_amdgcn_alignbit(hi.y, hi.y, 16), E1 = __builtin_amdgcn_alignbit(hi.x, hi.x, 16);
        const unsigned E2 = __builtin_amdgcn_alignbit(lo.w, lo.w, 16), E3 = __builtin_amdgcn_alignbit(lo.z, lo.z, 16);
        const unsigned E4 = __builtin_amdgcn_alignbit(lo.y, lo.y, 16);
        const unsigned O0 = (hi.y & 0xFFFFu) | (hi.x & 0xFFFF0000u), O1 = (hi.x & 0xFFFFu) | (lo.w & 0xFFFF0000u);
        const unsigned O2 = (lo.w & 0xFFFFu) | (lo.z & 0xFFFF0000u), O3 = (lo.z & 0xFFFFu) | (lo.y & 0xFFFF0000u);
        const unsigned O4 = (lo.y & 0xFFFFu) | (lo.x & 0xFFFF0000u);
        *(LAS v4u*)(cp + 3 * CPY + 16 * a) = v4u{E0, E1, E2, E3};
        *(LAS v4u*)(cp + 2 * CPY + 16 * a) = v4u{O0, O1, O2, O3};
        *(LAS v4u*)(cp + 1 * CPY + 16 * a) = v4u{E1, E2, E3, E4};
        *(LAS v4u*)(cp + 0 * CPY + 16 * a) = v4u{O1, O2, O3, O4};
      }
    }
  }
  STAMP(5);
  wait_flags(p.ws, WS_FUT + (g * 32 + (lane & 31)) * FL, lane < 32, tag);
  STAMP(6);
  {
    const int quad = 16 * g + (m >> 1);
    const int off = WS_UT + (quad * 256 + (tid >> 1)) * 64 + ((m & 1) * 2 + (tid & 1)) * 16;
    const v4u a0 = ld_sc1(rs, off), a1 = ld_sc1(rs, off + 128 * 64);
    LAS unsigned char* q = L + B_U + (tid & 1) * U_CH + 512 + (tid >> 1) * 16;
    *(LAS v4u*)q = a0;
    *(LAS v4u*)(q + 2048) = a1;
  }
  BAR();
  STAMP(7);
  f32x4 acc[8];
#pragma unroll
  for (int i = 0; i < 8; ++i) acc[i] = f32x4{0, 0, 0, 0};
  {
    LAS unsigned char* ab = L + ch * CPY_CH + (lr & 3) * CPY + 2 * (2032 - 1792 - 224 + 8 * lq - 4 * (lr >> 2));
    LAS unsigned char* ub = L + B_U + ch * U_CH + 512 + 2 * (16 * lr - 16 + 8 * lq - 224);
    ab += 2 * (224 - 32 * (w & 1));
    ub += 2 * (224 - 32 * (w & 1));
    v4u afA[8], bfA[8], afB[8], bfB[8];
    auto ldc = [&](v4u (&af)[8], v4u (&bf)[8], int rr) {
#pragma unroll
      for (int i = 0; i < 8; ++i) {
        bf[i] = *(LAS v4u*)(ub - 128 * rr + 512 * i);
        v2u x0, x1;
        asm volatile("" ::: "memory");
        x0 = *(LAS v2u*)(ab - 128 * rr + 512 * (7 - i));
        asm volatile("" ::: "memory");
        x1 = *(LAS v2u*)(ab - 128 * rr + 512 * (7 - i) + 8);
        af[i] = v4u{x0.x, x0.y, x1.x, x1.y};
      }
    };
    auto mmc = [&](v4u (&af)[8], v4u (&bf)[8]) {
#pragma unroll
      for (int sm = 0; sm < 8; ++sm)
#pragma unroll
        for (int o = 0; o <= sm; ++o) {
          if (sm + o < 8) acc[sm + o] = mfma_bf16(af[sm], bf[o], acc[sm + o]);
          if (o < sm && sm + o < 8) acc[sm + o] = mfma_bf16(af[o], bf[sm], acc[sm + o]);
        }
    };
    ldc(afA, bfA, 0);
    SB0();
#pragma unroll
    for (int rr = 0; rr < 4; rr += 2) {
      ldc(afB, bfB, rr + 1);
      mmc(afA, bfA);
#pragma unroll
      for (int i = 0; i < 24; ++i) { SGB(0x8, 1); SGB(0x100, 1); }
      SB0();
      if (rr + 2 < 4) ldc(afA, bfA, rr + 2);
      mmc(afB, bfB);
#pragma unroll
      for (int i = 0; i < 24; ++i) { SGB(0x8, 1); SGB(0x100, 1); }
      SB0();
    }
  }
  STAMP(8);
  {
    LAS unsigned char* X = L + 36864 + w * 4096;
    const int keep = (w & 1) * 4;
    f32x4 mine[4];
#pragma unroll
    for (int i = 0; i < 4; ++i) {
      f32x4 lo = acc[i], hi = acc[4 + i];
      asm("" : "+v"(lo), "+v"(hi));
      *(LAS f32x4*)(X + i * 1024 + lane * 16) = (w & 1) ? lo : hi;
      mine[i] = (w & 1) ? hi : lo;
    }
    BAR();
    LAS unsigned char* Y = L + 36864 + (w ^ 1) * 4096;
    const float dk = bf2f(r_dsk);
    v2u yo[4];
#pragma unroll
    for (int i = 0; i < 4; ++i) {
      const f32x4 s = mine[i] + *(LAS f32x4*)(Y + i * 1024 + lane * 16);
      const v2u uu = *(LAS v2u*)(L + B_U + ch * U_CH + 512 + 2 * (256 * (keep + i) + 16 * lr + 4 * lq));
      const unsigned y01 = pk_bf16(s[0], s[1]), y23 = pk_bf16(s[2], s[3]);
      const unsigned k01 = pk_bf16(bf2f(uu.x & 0xFFFFu) * dk, __uint_as_float(uu.x & 0xFFFF0000u) * dk);
      const unsigned k23 = pk_bf16(bf2f(uu.y & 0xFFFFu) * dk, __uint_as_float(uu.y & 0xFFFF0000u) * dk);
      yo[i].x = pk_bf16(bf2f(y01 & 0xFFFFu) + bf2f(k01 & 0xFFFFu), __uint_as_float(y01 & 0xFFFF0000u) + __uint_as_float(k01 & 0xFFFF0000u));
      yo[i].y = pk_bf16(bf2f(y23 & 0xFFFFu) + bf2f(k23 & 0xFFFFu), __uint_as_float(y23 & 0xFFFF0000u) + __uint_as_float(k23 & 0xFFFF0000u));
    }
#pragma unroll
    for (int i = 0; i < 4; i += 2) {
      auto r0 = __builtin_amdgcn_permlane16_swap(yo[i].x, yo[i + 1].x, false, false);
      auto r1 = __builtin_amdgcn_permlane16_swap(yo[i].y, yo[i + 1].y, false, false);
      const int win = keep + i + (lq & 1);
      st_sc1(rs, WS_YT + ((d * 8 + win) * 2 + (lq >> 1)) * 256 + lr * 16, v4u{r0[0], r1[0], r0[1], r1[1]});
    }
    DRAIN();
  }
  BAR();
  if (tid == 0) set_flag(p.ws, WS_FY + (g * 32 + m) * FL, tag);
  STAMP(9);
  wait_flags(p.ws, WS_FY + (g * 32 + (lane & 31)) * FL, lane < 32, tag);
  STAMP(10);
  {
    const int c0 = tid >> 3, hh = (tid >> 2) & 1, l4 = tid & 3;
    const int off = WS_YT + (((64 * g + c0) * 8 + (m >> 2)) * 2 + hh) * 256 + (4 * (m & 3) + l4) * 16;
    const v4u a0 = ld_sc1(rs, off), a1 = ld_sc1(rs, off + 32 * 4096);
    if (m == 0 && tid == 0) set_flag(p.ws, WS_EPB + 256 * g, tag);
    LAS unsigned char* q = L + c0 * 128 + l4 * 32 + hh * 16;
    *(LAS v4u*)q = a0;
    *(LAS v4u*)(q + 4096) = a1;
  }
  BAR();
  {
    LAS unsigned char* q = L + (8 * lq + ((lane >> 2) & 3)) * 128 + 32 * w + 8 * (lane & 3);
    GAS unsigned char* op = (GAS unsigned char*)(p.out + (size_t)(64 * m + 16 * w + lr) * 1024 + 512 + 64 * g + 8 * lq);
#pragma unroll
    for (int it = 0; it < 2; ++it) *(GAS v4u*)(op + it * 64) = tr_read2(q + it * 4096, q + it * 4096 + 512);
  }
  STAMP(11);
  STAMP_END(12);
}

__global__ void __launch_bounds__(256, 2) fused_kernel(Params p) {
  extern __shared__ __attribute__((aligned(16))) unsigned char smem[];
  LAS unsigned char* L = (LAS unsigned char*)smem;
  if (blockIdx.x >= 256) roleA(p, L, blockIdx.x - 256);
  else roleB(p, L, blockIdx.x);
}

extern "C" void kernel_launch(void* const* d_in, const int* in_sizes, int n_in, void* d_out, int out_size, void* d_ws,
                              size_t ws_size, hipStream_t stream) {
  Params p;
  p.x = (const u16*)d_in[0];
  p.wqkv = (const u16*)d_in[1];
  p.wo = (const u16*)d_in[2];
  p.u = (const u16*)d_in[3];
  p.lar = (const u16*)d_in[4];
  p.aim = (const u16*)d_in[5];
  p.cre = (const u16*)d_in[6];
  p.cim = (const u16*)d_in[7];
  p.ldt = (const u16*)d_in[8];
  p.dsk = (const u16*)d_in[9];
  p.out = (u16*)d_out;
  p.ws = (unsigned char*)d_ws;
  (void)hipFuncSetAttribute((const void*)fused_kernel, hipFuncAttributeMaxDynamicSharedMemorySize, LDS_BYTES);
  fused_kernel<<<512, 256, LDS_BYTES, stream>>>(p);
}
```

```cpp
#include <hip/hip_runtime.h>
#include <stdint.h>

#define LAS __attribute__((address_space(3)))
#define GAS __attribute__((address_space(1)))
#define DEV __device__ __forceinline__
typedef unsigned short u16;
typedef unsigned long long u64;
typedef unsigned v4u __attribute__((ext_vector_type(4)));
typedef unsigned v2u __attribute__((ext_vector_type(2)));
typedef short s16x4 __attribute__((ext_vector_type(4)));
typedef __bf16 bf16x8 __attribute__((ext_vector_type(8)));
typedef __bf16 bf16x2 __attribute__((ext_vector_type(2)));
typedef _Float16 f16x2 __attribute__((ext_vector_type(2)));
typedef _Float16 f16x8 __attribute__((ext_vector_type(8)));
typedef float f32x4 __attribute__((ext_vector_type(4)));
typedef float f32x2 __attribute__((ext_vector_type(2)));
typedef GAS u64 gu64;

#define STAMPS 0

constexpr int FL = 128;
constexpr int WS_EPA = 0;
constexpr int WS_EPB = 256;
constexpr int WS_FKV = 4096;
constexpr int WS_FUT = WS_FKV + 256 * FL;
constexpr int WS_FY = WS_FUT + 256 * FL;
constexpr int WS_FO = WS_FY + 256 * FL;
constexpr int KVP_REC = 2304;
constexpr int WS_KVP = WS_FO + 1024 * FL;
constexpr int WS_O = WS_KVP + 256 * KVP_REC;
constexpr int WS_UT = WS_O + 2 * 1024 * 1024;
constexpr int WS_YT = WS_UT + 2 * 1024 * 1024;
constexpr int WS_STAMP = WS_YT + 2 * 1024 * 1024;
constexpr int WS_BYTES = WS_STAMP + 512 * 4 * 16 * 8;
constexpr int LDS_BYTES = 80 * 1024;
constexpr unsigned SPIN_CAP = 1u << 18;

struct Params {
  const u16 *x, *wqkv, *wo, *u, *lar, *aim, *cre, *cim, *ldt, *dsk;
  u16* out;
  unsigned char* ws;
};

DEV float bf2f(unsigned v) { return __uint_as_float(v << 16); }
DEV unsigned pk_bf16(float a, float b) {
  f32x2 v = {a, b};
  return __builtin_bit_cast(unsigned, __builtin_convertvector(v, bf16x2));
}
DEV unsigned pk_f16(float a, float b) {
  f32x2 v = {a, b};
  return __builtin_bit_cast(unsigned, __builtin_convertvector(v, f16x2));
}
DEV float rbf(float a) { return __uint_as_float(pk_bf16(a, a) << 16); }
DEV f32x4 mfma_bf16(v4u a, v4u b, f32x4 c) {
  return __builtin_amdgcn_mfma_f32_16x16x32_bf16(__builtin_bit_cast(bf16x8, a), __builtin_bit_cast(bf16x8, b), c, 0, 0, 0);
}
DEV f32x4 mfma_f16(v4u a, v4u b, f32x4 c) {
  return __builtin_amdgcn_mfma_f32_16x16x32_f16(__builtin_bit_cast(f16x8, a), __builtin_bit_cast(f16x8, b), c, 0, 0, 0);
}
DEV f32x4 mfma16_bf16(v2u a, v2u b, f32x4 c) {
  return __builtin_amdgcn_mfma_f32_16x16x16bf16_1k(__builtin_bit_cast(s16x4, a), __builtin_bit_cast(s16x4, b), c, 0, 0, 0);
}
DEV v2u tr_read(LAS unsigned char* p) {
  return __builtin_bit_cast(v2u, __builtin_amdgcn_ds_read_tr16_b64_v4i16((LAS s16x4*)p));
}
DEV v4u tr_read2(LAS unsigned char* p0, LAS unsigned char* p1) {
  v2u a = tr_read(p0), b = tr_read(p1);
  return v4u{a.x, a.y, b.x, b.y};
}
DEV void glds16(const void* sbase, unsigned voff, unsigned lds_dst) {
  unsigned keep;
  asm volatile("s_mov_b32 %0, m0\n\ts_mov_b32 m0, %3\n\ts_nop 0\n\tglobal_load_lds_dwordx4 %1, %2\n\ts_mov_b32 m0, %0"
               : "=&s"(keep) : "v"(voff), "s"(sbase), "s"(lds_dst) : "memory");
}
#define DRAIN() asm volatile("s_waitcnt vmcnt(0)" ::: "memory")
#define BAR() do { asm volatile("s_waitcnt lgkmcnt(0)" ::: "memory"); __builtin_amdgcn_s_barrier(); asm volatile("" ::: "memory"); } while (0)
#define RLX_AGENT __ATOMIC_RELAXED, __HIP_MEMORY_SCOPE_AGENT
constexpr int AUX_SC1 = 16;
typedef __amdgpu_buffer_rsrc_t rsrc_t;
DEV v4u ld_sc1(rsrc_t rs, int off) { return __builtin_amdgcn_raw_buffer_load_b128(rs, off, 0, AUX_SC1); }
DEV void st_sc1(rsrc_t rs, int off, v4u v) { __builtin_amdgcn_raw_buffer_store_b128(v, rs, off, 0, AUX_SC1); }
DEV void wait_flags(unsigned char* ws, int off, bool active, u64 tag) {
  gu64* f = (gu64*)(ws + off);
#pragma unroll 1
  for (unsigned spins = 0; spins < SPIN_CAP; ++spins) {
    bool ok = true;
    if (active) ok = __hip_atomic_load(f, RLX_AGENT) == tag;
    if (__all(ok)) break;
    __builtin_amdgcn_s_sleep(1);
  }
  __builtin_amdgcn_fence(__ATOMIC_ACQUIRE, "wavefront");
}
DEV void set_flag(unsigned char* ws, int off, u64 tag) { __hip_atomic_store((gu64*)(ws + off), tag, RLX_AGENT); }

#define STAMP_DECL
#define STAMP(k) do { } while (0)
#define STAMP_END(n) do { } while (0)

DEV float phi(float t) {
  float e = __builtin_amdgcn_exp2f(t * 1.44269504088896341f);
  e = __builtin_amdgcn_fmed3f(e, 0.f, 1.f);
  return (fmaxf(t, 0.f) + e) + 1e-6f;
}
template <int V> struct IC { static constexpr int value = V; };
struct cf { float x, y; };
DEV cf cmul(cf a, cf b) {
  cf r;
  r.x = __builtin_fmaf(-a.y, b.y, a.x * b.x);
  r.y = __builtin_fmaf(a.y, b.x, a.x * b.y);
  asm("" : "+v"(r.x));
  return r;
}
DEV void gld16(v4u& dst, const void* sbase, unsigned voff) {
  asm volatile("global_load_dwordx4 %0, %1, %2" : "=v"(dst) : "v"(voff), "s"(sbase) : "memory");
}
DEV void gld16_sc1(v4u& dst, const void* sbase, unsigned voff) {
  asm volatile("global_load_dwordx4 %0, %1, %2 sc1" : "=v"(dst) : "v"(voff), "s"(sbase) : "memory");
}
DEV unsigned touch(const void* ptr) {
  unsigned t;
  asm volatile("global_load_dword %0, %1, off sc1" : "=v"(t) : "v"(ptr) : "memory");
  return t;
}
template <int N> DEV void vmwait4(v4u (&r)[4]) {
  asm volatile("s_waitcnt vmcnt(%4)" : "+v"(r[0]), "+v"(r[1]), "+v"(r[2]), "+v"(r[3]) : "n"(N) : "memory");
}
template <int N> DEV void vmwait() { asm volatile("s_waitcnt vmcnt(%0)" ::"n"(N) : "memory"); }
#define SB0() __builtin_amdgcn_sched_barrier(0)
#define SGB(mask, n) __builtin_amdgcn_sched_group_barrier(mask, n, 0)

DEV void roleA(const Params& p, LAS unsigned char* L, const int a) {
  const int tid = threadIdx.x, lane = tid & 63, w = __builtin_amdgcn_readfirstlane(tid >> 6);
  const int lr = lane & 15, lq = lane >> 4;
  const int g = a & 7, m = a >> 3;
  const int bi = 4 * (g >> 1) + (m >> 3), bj = 8 * (g & 1) + (m & 7);
  const unsigned Lb = (unsigned)(uintptr_t)L;
  const rsrc_t rs = __builtin_amdgcn_make_buffer_rsrc(p.ws, 0, WS_BYTES, 0x00020000);
  STAMP_DECL
  STAMP(0);
  const int rot = ((m & 7) + 2 * (m >> 3)) & 7;
  const unsigned wvoff = (unsigned)(((16 * w + (lane >> 2)) * 1536 + 32 * bj + 8 * ((lane & 3) ^ ((lane >> 5) << 1))) * 2);
  const unsigned xvoff = (unsigned)((128 * bi + 32 * w + (lane >> 3)) * 1024 + (((lane & 7) ^ (lane >> 3)) * 16));
  auto issue = [&](int c) {
    const int kc = ((c + rot) & 7) * 64;
#pragma unroll
    for (int sg = 0; sg < 2; ++sg)
      glds16(p.wqkv + (size_t)kc * 1536 + 512 * (1 + sg), wvoff, Lb + 49152 + (c % 3) * 8192 + sg * 4096 + w * 1024);
#pragma unroll
    for (int r = 0; r < 4; ++r) glds16(p.x + 8 * r * 512 + kc, xvoff, Lb + (c % 3) * 16384 + w * 4096 + r * 1024);
  };
  auto issue_q = [&](int h) {
#pragma unroll
    for (int c = 0; c < 4; ++c)
      glds16(p.wqkv + (size_t)(((4 * h + c + rot) & 7) * 64) * 1536, wvoff, Lb + ((2 + h) % 3) * 16384 + c * 4096 + w * 1024);
  };
  const u64 tag = __hip_atomic_load((gu64*)(p.ws + WS_EPA), RLX_AGENT) + 1;
  unsigned tch;
  {
    const int R_ = 4 * g + (m >> 3), C_ = m & 7;
    const unsigned char* tp;
    if (w == 0) tp = (const unsigned char*)(p.out + (size_t)(64 * R_ + lane) * 1024 + 64 * C_);
    else if (w == 1) tp = p.ws + WS_O + ((lane < 16) ? (4 * R_ * 16) * 1024 + lane * 4096 : ((8 * bi + (lane & 7)) * 16 + bj) * 1024);
    else if (w == 2) tp = p.ws + ((lane < 16) ? WS_KVP + (bj * 16 + lane) * KVP_REC : (lane < 32) ? WS_FKV + (bj * 16 + lr) * FL : (lane < 48) ? WS_FO + (2 * R_ * 16 + 2 * lr) * FL : WS_FO + ((bi * 4 + (lane & 3)) * 16 + bj) * FL);
    else tp = (const unsigned char*)(p.wo + (size_t)(8 * lane) * 512 + 64 * C_);
    tch = touch(tp);
  }
  issue(0);
  issue(1);
  SB0();
  STAMP(1);
  LAS unsigned char* tb0 = L + (8 * lq + ((lane >> 2) & 3)) * 64 + 32 * (lq & 1) + 8 * (lane & 3);
  LAS unsigned char* tb1 = L + (8 * lq + ((lane >> 2) & 3)) * 64 + 32 * ((lq & 1) ^ 1) + 8 * (lane & 3);
  LAS unsigned char* xr0 = L + w * 4096 + lr * 128 + ((lq ^ (lr & 7)) * 16);
  LAS unsigned char* xr1 = L + w * 4096 + lr * 128 + (((4 + lq) ^ (lr & 7)) * 16);
  v4u xf[8][4];
  f32x4 ak[2][2], av[2][2];
#pragma unroll
  for (int r = 0; r < 2; ++r)
#pragma unroll
    for (int h = 0; h < 2; ++h) { ak[r][h] = f32x4{0, 0, 0, 0}; av[r][h] = f32x4{0, 0, 0, 0}; }
  auto kv_chunk = [&](auto cc) {
    constexpr int c = decltype(cc)::value;
    constexpr int wo_ = 49152 + (c % 3) * 8192, xo_ = (c % 3) * 16384;
    vmwait<(c < 7) ? 6 : 4>();
    __builtin_amdgcn_s_barrier();
    asm volatile("" ::: "memory");
    v4u wa[4], wb[4];
    xf[c][0] = *(LAS v4u*)(xr0 + xo_);
    wa[0] = tr_read2(tb0 + wo_, tb0 + wo_ + 256);
    wa[1] = tr_read2(tb1 + wo_, tb1 + wo_ + 256);
    xf[c][1] = *(LAS v4u*)(xr0 + xo_ + 2048);
    wa[2] = tr_read2(tb0 + wo_ + 4096, tb0 + wo_ + 4096 + 256);
    wa[3] = tr_read2(tb1 + wo_ + 4096, tb1 + wo_ + 4096 + 256);
    SB0();
    if constexpr (c + 2 < 8) issue(c + 2); else issue_q(c - 6);
    SB0();
    xf[c][2] = *(LAS v4u*)(xr1 + xo_);
    wb[0] = tr_read2(tb0 + wo_ + 2048, tb0 + wo_ + 2048 + 256);
    wb[1] = tr_read2(tb1 + wo_ + 2048, tb1 + wo_ + 2048 + 256);
    xf[c][3] = *(LAS v4u*)(xr1 + xo_ + 2048);
    wb[2] = tr_read2(tb0 + wo_ + 4096 + 2048, tb0 + wo_ + 4096 + 2048 + 256);
    wb[3] = tr_read2(tb1 + wo_ + 4096 + 2048, tb1 + wo_ + 4096 + 2048 + 256);
#pragma unroll
    for (int r = 0; r < 2; ++r) {
      ak[r][0] = mfma_bf16(xf[c][r], wa[0], ak[r][0]);
      ak[r][1] = mfma_bf16(xf[c][r], wa[1], ak[r][1]);
      av[r][0] = mfma_bf16(xf[c][r], wa[2], av[r][0]);
      av[r][1] = mfma_bf16(xf[c][r], wa[3], av[r][1]);
    }
#pragma unroll
    for (int i = 0; i < 5; ++i) { SGB(0x8, 1); SGB(0x100, 2); }
    SB0();
#pragma unroll
    for (int r = 0; r < 2; ++r) {
      ak[r][0] = mfma_bf16(xf[c][2 + r], wb[0], ak[r][0]);
      ak[r][1] = mfma_bf16(xf[c][2 + r], wb[1], ak[r][1]);
      av[r][0] = mfma_bf16(xf[c][2 + r], wb[2], av[r][0]);
      av[r][1] = mfma_bf16(xf[c][2 + r], wb[3], av[r][1]);
    }
    SB0();
  };
  kv_chunk(IC<0>{}); kv_chunk(IC<1>{}); kv_chunk(IC<2>{}); kv_chunk(IC<3>{});
  kv_chunk(IC<4>{}); kv_chunk(IC<5>{}); kv_chunk(IC<6>{}); kv_chunk(IC<7>{});
  STAMP(2);
  LAS unsigned char* S = L + 73728;
  LAS unsigned char* SZ = L + 49152;
  {
    const v4u ones = {0x3F803F80u, 0x3F803F80u, 0x3F803F80u, 0x3F803F80u};
#pragma unroll
    for (int h = 0; h < 2; ++h) {
      v4u pk, vv;
      pk.x = pk_bf16(phi(ak[0][h][0]), phi(ak[0][h][1]));
      pk.y = pk_bf16(phi(ak[0][h][2]), phi(ak[0][h][3]));
      pk.z = pk_bf16(phi(ak[1][h][0]), phi(ak[1][h][1]));
      pk.w = pk_bf16(phi(ak[1][h][2]), phi(ak[1][h][3]));
      vv.x = pk_bf16(av[0][h][0], av[0][h][1]);
      vv.y = pk_bf16(av[0][h][2], av[0][h][3]);
      vv.z = pk_bf16(av[1][h][0], av[1][h][1]);
      vv.w = pk_bf16(av[1][h][2], av[1][h][3]);
      const f32x4 kv = mfma_bf16(pk, vv, f32x4{0, 0, 0, 0});
      const f32x4 zz = mfma_bf16(pk, ones, f32x4{0, 0, 0, 0});
      *(LAS f32x4*)(S + w * 2048 + h * 1024 + lane * 16) = kv;
      if (lr == 0) *(LAS f32x4*)(SZ + w * 128 + h * 64 + lq * 16) = zz;
    }
  }
  asm volatile("" ::"v"(tch));
  asm volatile("" ::"v"(tag));
  DRAIN();
  BAR();
  if (w == 0) {
    const int rec = WS_KVP + (bj * 16 + bi) * KVP_REC;
    f32x4 s0 = *(LAS f32x4*)(S + lane * 16), s1 = *(LAS f32x4*)(S + 1024 + lane * 16);
    f32x4 sz = *(LAS f32x4*)(SZ + (lane & 7) * 16);
#pragma unroll
    for (int ww = 1; ww < 4; ++ww) {
      s0 += *(LAS f32x4*)(S + ww * 2048 + lane * 16);
      s1 += *(LAS f32x4*)(S + ww * 2048 + 1024 + lane * 16);
      sz += *(LAS f32x4*)(SZ + ww * 128 + (lane & 7) * 16);
    }
    st_sc1(rs, rec + lane * 16, __builtin_bit_cast(v4u, s0));
    st_sc1(rs, rec + 1024 + lane * 16, __builtin_bit_cast(v4u, s1));
    if (lane < 8) st_sc1(rs, rec + 2048 + lane * 16, __builtin_bit_cast(v4u, sz));
    DRAIN();
    if (lane == 0) set_flag(p.ws, WS_FKV + (bj * 16 + bi) * FL, tag);
  }
  STAMP(3);
  const int R = 4 * g + (m >> 3), C = m & 7;
  const unsigned ovoff = (unsigned)(((lane >> 3) * 512 + 64 * C + 8 * ((lane & 7) ^ (((lane >> 4) & 3) << 1))) * 2);
#pragma unroll
  for (int n = 0; n < 8; ++n) glds16(p.wo + (size_t)(64 * w + 8 * n) * 512, ovoff, Lb + ((w < 2) ? 16384 + w * 8192 : 57344 + (w - 2) * 8192) + n * 1024);
  f32x4 aq[2][2];
#pragma unroll
  for (int r = 0; r < 2; ++r)
#pragma unroll
    for (int h = 0; h < 2; ++h) aq[r][h] = f32x4{0, 0, 0, 0};
  {
    v4u qa[4], qb[4];
    auto ldq = [&](v4u (&q)[4], int c) {
      const int o = (c < 4) ? 32768 + c * 4096 : (c - 4) * 4096;
      q[0] = tr_read2(tb0 + o, tb0 + o + 256);
      q[1] = tr_read2(tb1 + o, tb1 + o + 256);
      q[2] = tr_read2(tb0 + o + 2048, tb0 + o + 2048 + 256);
      q[3] = tr_read2(tb1 + o + 2048, tb1 + o + 2048 + 256);
    };
    auto mmq = [&](v4u (&q)[4], int c) {
#pragma unroll
      for (int kk = 0; kk < 2; ++kk)
#pragma unroll
        for (int r = 0; r < 2; ++r) {
          aq[r][0] = mfma_bf16(q[2 * kk], xf[c][2 * kk + r], aq[r][0]);
          aq[r][1] = mfma_bf16(q[2 * kk + 1], xf[c][2 * kk + r], aq[r][1]);
        }
    };
    ldq(qa, 0);
    SB0();
#pragma unroll
    for (int c = 0; c < 8; c += 2) {
      ldq(qb, c + 1);
      mmq(qa, c);
#pragma unroll
      for (int i = 0; i < 8; ++i) { SGB(0x8, 1); SGB(0x100, 1); }
      SB0();
      if (c + 2 < 8) ldq(qa, c + 2);
      mmq(qb, c + 1);
#pragma unroll
      for (int i = 0; i < 8; ++i) { SGB(0x8, 1); SGB(0x100, 1); }
      SB0();
    }
  }
  v2u pq[2][2];
#pragma unroll
  for (int r = 0; r < 2; ++r)
#pragma unroll
    for (int h = 0; h < 2; ++h) {
      pq[r][h].x = pk_bf16(phi(aq[r][h][0]), phi(aq[r][h][1]));
      pq[r][h].y = pk_bf16(phi(aq[r][h][2]), phi(aq[r][h][3]));
    }
  BAR();
  STAMP(4);
  wait_flags(p.ws, WS_FKV + (bj * 16 + 4 * w + (lane & 3)) * FL, lane < 4, tag);
  STAMP(5);
  {
    v4u t0[4], t1[4], tz[4];
#pragma unroll
    for (int q = 0; q < 4; ++q) {
      const int rec = WS_KVP + (bj * 16 + 4 * w + q) * KVP_REC;
      gld16_sc1(t0[q], p.ws + rec, lane * 16);
      gld16_sc1(t1[q], p.ws + rec + 1024, lane * 16);
      gld16_sc1(tz[q], p.ws + rec + 2048, (lane & 7) * 16);
    }
    SB0();
#pragma unroll
    for (int n = 0; n < 8; ++n) glds16(p.wo + (size_t)(256 + 64 * w + 8 * n) * 512, ovoff, Lb + ((w < 2) ? w * 8192 : 32768 + (w - 2) * 8192) + n * 1024);
    asm volatile("s_waitcnt vmcnt(8)" : "+v"(t0[0]), "+v"(t0[1]), "+v"(t0[2]), "+v"(t0[3]), "+v"(t1[0]), "+v"(t1[1]), "+v"(t1[2]), "+v"(t1[3]), "+v"(tz[0]), "+v"(tz[1]), "+v"(tz[2]), "+v"(tz[3]));
    f32x4 s0 = __builtin_bit_cast(f32x4, t0[0]), s1 = __builtin_bit_cast(f32x4, t1[0]), sz = __builtin_bit_cast(f32x4, tz[0]);
#pragma unroll
    for (int q = 1; q < 4; ++q) {
      s0 += __builtin_bit_cast(f32x4, t0[q]);
      s1 += __builtin_bit_cast(f32x4, t1[q]);
      sz += __builtin_bit_cast(f32x4, tz[q]);
    }
    *(LAS f32x4*)(S + w * 2048 + lane * 16) = s0;
    *(LAS f32x4*)(S + w * 2048 + 1024 + lane * 16) = s1;
    if (lane < 8) *(LAS f32x4*)(SZ + w * 128 + lane * 16) = sz;
  }
  BAR();
  STAMP(6);
  {
    v2u kvb[2], zb[2];
#pragma unroll
    for (int h = 0; h < 2; ++h) {
      f32x4 s = *(LAS f32x4*)(S + h * 1024 + lane * 16);
      f32x4 z = *(LAS f32x4*)(SZ + h * 64 + lq * 16);
#pragma unroll
      for (int ww = 1; ww < 4; ++ww) {
        s += *(LAS f32x4*)(S + ww * 2048 + h * 1024 + lane * 16);
        z += *(LAS f32x4*)(SZ + ww * 128 + h * 64 + lq * 16);
      }
      kvb[h] = v2u{pk_bf16(s[0], s[1]), pk_bf16(s[2], s[3])};
      zb[h] = v2u{pk_bf16(z[0], z[1]), pk_bf16(z[2], z[3])};
    }
#pragma unroll
    for (int r = 0; r < 2; ++r) {
      v4u ov;
#pragma unroll
      for (int h = 0; h < 2; ++h) {
        const f32x4 num = mfma16_bf16(kvb[h], pq[r][h], f32x4{0, 0, 0, 0});
        const f32x4 den = mfma16_bf16(zb[h], pq[r][h], f32x4{0, 0, 0, 0});
        const float rd = __builtin_amdgcn_rcpf(den[0] + 1e-6f);
        const unsigned lo = pk_bf16(num[0] * rd, num[1] * rd), hi = pk_bf16(num[2] * rd, num[3] * rd);
        if (h == 0) { ov.x = lo; ov.y = hi; } else { ov.z = lo; ov.w = hi; }
      }
      st_sc1(rs, WS_O + ((8 * bi + 2 * w + r) * 16 + bj) * 1024 + lane * 16, ov);
    }
    DRAIN();
    if (lane == 0) set_flag(p.ws, WS_FO + ((bi * 4 + w) * 16 + bj) * FL, tag);
  }
  STAMP(7);
  const int T = 4 * R + w;
  wait_flags(p.ws, WS_FO + ((T >> 1) * 16 + lr) * FL, lane < 16, tag);
  STAMP(8);
  v4u of[16];
#pragma unroll
  for (int j = 0; j < 16; ++j) of[j] = ld_sc1(rs, WS_O + (T * 16 + j) * 1024 + lane * 16);
  if (a == 0 && tid == 0) set_flag(p.ws, WS_EPA, tag);
  __builtin_amdgcn_sched_barrier(0);
  asm volatile("" ::"v"(of[0]));
  BAR();
  STAMP(9);
  f32x4 acc[4];
#pragma unroll
  for (int c = 0; c < 4; ++c) acc[c] = f32x4{0, 0, 0, 0};
  {
    const int ra = 4 * lq + ((lane >> 2) & 3);
    const int sw = (ra >> 1) & 3;
    LAS unsigned char* ob = L + 128 * ra + 8 * (lane & 3);
    v4u fa[4], fb[4];
    auto ldw = [&](v4u (&f)[4], int j) {
      const int off = (j < 4) ? 16384 + j * 4096 : (j < 8) ? 57344 + (j - 4) * 4096 : (j < 12) ? (j - 8) * 4096 : 32768 + (j - 12) * 4096;
#pragma unroll
      for (int c = 0; c < 4; ++c) {
        LAS unsigned char* q = ob + 32 * (c ^ sw) + off;
        f[c] = tr_read2(q, q + 2048);
      }
    };
    ldw(fa, 0);
    SB0();
#pragma unroll
    for (int j = 0; j < 16; j += 2) {
      ldw(fb, j + 1);
#pragma unroll
      for (int c = 0; c < 4; ++c) acc[c] = mfma_bf16(fa[c], of[j], acc[c]);
#pragma unroll
      for (int i = 0; i < 4; ++i) { SGB(0x8, 1); SGB(0x100, 2); }
      SB0();
      if (j + 2 < 16) ldw(fa, j + 2);
#pragma unroll
      for (int c = 0; c < 4; ++c) acc[c] = mfma_bf16(fb[c], of[j + 1], acc[c]);
#pragma unroll
      for (int i = 0; i < 4; ++i) { SGB(0x8, 1); SGB(0x100, 2); }
      SB0();
    }
  }
  STAMP(10);
  {
    GAS unsigned char* op = (GAS unsigned char*)(p.out + (size_t)(16 * T + lr) * 1024 + 64 * C) + (lq & 1) * 32 + (lq >> 1) * 16;
#pragma unroll
    for (int c = 0; c < 4; c += 2) {
      unsigned a0 = pk_bf16(acc[c][0], acc[c][1]), a1 = pk_bf16(acc[c][2], acc[c][3]);
      unsigned b0 = pk_bf16(acc[c + 1][0], acc[c + 1][1]), b1 = pk_bf16(acc[c + 1][2], acc[c + 1][3]);
      auto r0 = __builtin_amdgcn_permlane16_swap(a0, b0, false, false);
      auto r1 = __builtin_amdgcn_permlane16_swap(a1, b1, false, false);
      *(GAS v4u*)(op + c * 32) = v4u{r0[0], r1[0], r0[1], r1[1]};
    }
  }
  STAMP(11);
  STAMP_END(12);
}

constexpr int TROW = 528;
constexpr int TAB_CH = 57 * TROW;
constexpr int B_TAB = 0;
constexpr int B_KX = 60416;
constexpr int KX_CH = 4352;
constexpr int B_U = B_KX + 2 * KX_CH;
constexpr int U_CH = 4608;
constexpr int CPY = 4160;
constexpr int CPY_CH = 4 * CPY;
static_assert(B_U + 2 * U_CH <= LDS_BYTES, "lds");

DEV cf cpow_direct(float al, float be, float pw) {
  const float mag = __builtin_amdgcn_exp2f(al * pw);
  const float ph = __builtin_amdgcn_fractf(be * pw);
  return cf{mag * __builtin_amdgcn_cosf(ph), mag * __builtin_amdgcn_sinf(ph)};
}

DEV void roleB(const Params& p, LAS unsigned char* L, const int b) {
  const int tid = threadIdx.x, lane = tid & 63, w = __builtin_amdgcn_readfirstlane(tid >> 6);
  const int lr = lane & 15, lq = lane >> 4;
  const int g = b & 7, m = b >> 3;
  const int ch = w >> 1;
  const int d = 64 * g + 2 * m + ch;
  const rsrc_t rs = __builtin_amdgcn_make_buffer_rsrc(p.ws, 0, WS_BYTES, 0x00020000);
  STAMP_DECL
  STAMP(0);
  const GAS v4u* up = (const GAS v4u*)(p.u + (size_t)(64 * m + (tid >> 3)) * 512 + 64 * g + 8 * (tid & 7));
  const v4u ut0 = up[0], ut1 = up[32 * 64];
  const u64 tag = __hip_atomic_load((gu64*)(p.ws + WS_EPB + 256 * g), RLX_AGENT) + 1;
  const int n = tid & 127;
  const unsigned r_lar = p.lar[d * 128 + n], r_aim = p.aim[d * 128 + n];
  const unsigned r_cre = p.cre[d * 128 + n], r_cim = p.cim[d * 128 + n];
  const unsigned r_cre2 = p.cre[d * 128 + (n ^ 64)], r_cim2 = p.cim[d * 128 + (n ^ 64)];
  const unsigned r_ldt = p.ldt[d], r_dsk = p.dsk[d];
  unsigned tch;
  {
    const unsigned char* tp;
    if (w == 0) tp = (const unsigned char*)(p.out + (size_t)(64 * m + lane) * 1024 + 512 + 64 * g);
    else if (w == 1) tp = p.ws + WS_UT + ((lane < 16) ? ((16 * g + lane) * 256 + 8 * m) * 64 : (16 * g + (m >> 1)) * 16384 + (lane & 3) * 4096);
    else if (w == 2) tp = p.ws + WS_YT + (64 * g + lane) * 4096 + (m >> 2) * 512;
    else tp = p.ws + ((lane < 32) ? WS_FUT + (g * 32 + lane) * FL : WS_FY + (g * 32 + lane - 32) * FL);
    tch = touch(tp);
  }
  {
    unsigned t;
    if (w == 0) t = *(const GAS unsigned*)(p.wqkv + (size_t)(16 * m + (lane >> 2)) * 1536 + 256 * (g & 1) + 64 * (lane & 3));
    else t = *(const GAS unsigned*)(p.wo + (size_t)(m * 128 + ((w - 1) & 1) * 64 + lane) * 64);
    asm volatile("" ::"v"(t), "v"(tch));
  }
  *(LAS v4u*)(L + (tid >> 3) * 128 + (tid & 7) * 16) = ut0;
  *(LAS v4u*)(L + 4096 + (tid >> 3) * 128 + (tid & 7) * 16) = ut1;
  if (tid < 64) *(LAS v4u*)(L + B_U + (tid >> 5) * U_CH + (tid & 31) * 16) = v4u{0, 0, 0, 0};
  else if (tid < 68) *(LAS v4u*)(L + B_KX + ((tid >> 1) & 1) * KX_CH + (tid & 1) * 16) = v4u{0, 0, 0, 0};
  BAR();
  STAMP(1);
  {
    LAS unsigned char* q = L + (8 * lq + ((lane >> 2) & 3)) * 128 + 32 * w + 8 * (lane & 3);
#pragma unroll
    for (int it = 0; it < 2; ++it) {
      const v4u v = tr_read2(q + it * 4096, q + it * 4096 + 512);
      const int quad = 16 * g + 4 * w + (lr >> 2), oct = 8 * m + 4 * it + lq;
      st_sc1(rs, WS_UT + (quad * 256 + oct) * 64 + (lr & 3) * 16, v);
    }
    DRAIN();
  }
  BAR();
  if (tid == 0) set_flag(p.ws, WS_FUT + (g * 32 + m) * FL, tag);
  STAMP(2);
  float kscale;
  {
    const float dt = rbf(expf(bf2f(r_ldt)));
    const float ar = -rbf(expf(bf2f(r_lar))) * dt;
    const float al = ar * 1.44269504088896341f;
    const float be = (bf2f(r_aim) * dt) * 0.15915494309189535f;
    unsigned mx = max(max(r_cre & 0x7FFFu, r_cim & 0x7FFFu), max(r_cre2 & 0x7FFFu, r_cim2 & 0x7FFFu));
#pragma unroll
    for (int o = 32; o >= 1; o >>= 1) mx = max(mx, (unsigned)__shfl_xor((int)mx, o));
    int ex = (int)(mx >> 7);
    ex = min(max(ex, 1), 253);
    const float cs = __uint_as_float((unsigned)(254 - ex) << 23);
    kscale = 2.f * dt * __uint_as_float((unsigned)ex << 23);
    const cf z1 = cpow_direct(al, be, 1.f), z8 = cpow_direct(al, be, 8.f), z128 = cpow_direct(al, be, 128.f);
    const cf z1024 = cpow_direct(al, be, 1024.f);
    LAS unsigned char* tp = L + B_TAB + ch * TAB_CH + n * 4;
#define PUTG(row, v) do { const cf v_ = (v); *(LAS unsigned*)(tp + (row) * TROW) = pk_f16(v_.x, v_.y); } while (0)
    cf zp[8];
    zp[0] = cf{1.f, 0.f};
    zp[1] = z1;
#pragma unroll
    for (int i = 2; i < 8; ++i) zp[i] = cmul(zp[i - 1], z1);
#pragma unroll
    for (int i = 0; i < 8; ++i) PUTG(i, zp[i]);
    cf f = cf{bf2f(r_cre) * cs, bf2f(r_cim) * cs};
    cf h = z128, h8 = z1024;
#pragma unroll
    for (int i = 0; i < 16; ++i) {
      *(LAS unsigned*)(tp + (41 + i) * TROW) = pk_f16(f.x, -f.y);
      if (i < 15) f = cmul(f, z8);
      if (i < 9) {
        PUTG(32 + i, h8);
        if (i < 8) h8 = cmul(h8, z128);
      }
      if (i >= 1 && i <= 7) {
        if (i == 1) {
          PUTG(8, h);
#pragma unroll
          for (int q = 1; q < 8; ++q) PUTG(8 + q, cmul(h, zp[q]));
        } else if (i <= 3) {
          PUTG(16 + 4 * (i - 2), h);
#pragma unroll
          for (int q = 1; q < 4; ++q) PUTG(16 + 4 * (i - 2) + q, cmul(h, zp[2 * q]));
        } else {
          PUTG(24 + 2 * (i - 4), h);
          PUTG(24 + 2 * (i - 4) + 1, cmul(h, zp[4]));
        }
        if (i < 7) h = cmul(h, z128);
      }
    }
#undef PUTG
  }
  BAR();
  STAMP(3);
  {
    LAS unsigned char* tb = L + B_TAB + ch * TAB_CH + lq * 16;
    LAS unsigned char* kx = L + B_KX + ch * KX_CH + 32;
    LAS unsigned char* fb = tb + (41 + lr) * TROW;
    if ((w & 1) == 0) {
      LAS unsigned char* g0 = tb + lr * TROW;
      LAS unsigned char* g2 = tb + (32 + min(lr, 8)) * TROW;
      f32x4 c0 = {0, 0, 0, 0}, c2 = {0, 0, 0, 0};
#pragma unroll
      for (int s = 0; s < 8; ++s) {
        const v4u fv = *(LAS v4u*)(fb + s * 64);
        c0 = mfma_f16(*(LAS v4u*)(g0 + s * 64), fv, c0);
        c2 = mfma_f16(*(LAS v4u*)(g2 + s * 64), fv, c2);
      }
      c0 *= kscale;
      c2 *= kscale;
      *(LAS v2u*)(kx + 2 * (128 * (lq >> 1) + 8 * lr + 4 * (lq & 1))) = v2u{pk_bf16(c0[0], c0[1]), pk_bf16(c0[2], c0[3])};
#pragma unroll
      for (int r = 0; r < 4; ++r) {
        const unsigned vv = pk_bf16(c2[r], c2[r]);
        const unsigned pv = (unsigned)__builtin_amdgcn_update_dpp(0, (int)vv, 0x111, 0xf, 0xf, false);
        const unsigned first = (lr & 1) ? ((pv & 0xFFFFu) | (vv & 0xFFFF0000u)) : vv;
        const int pp = 128 * (8 + 4 * lq + r) + 8 * lr;
        if (lq < 2) {
          if (pp != 1024) *(LAS unsigned*)(kx + 2 * (pp - 4)) = first;
          *(LAS unsigned*)(kx + 2 * (pp - 2)) = vv;
          *(LAS v2u*)(kx + 2 * pp) = v2u{vv, vv};
        } else if (pp == 2048) {
          *(LAS v2u*)(kx + 2 * (pp - 4)) = v2u{vv, vv};
        }
      }
    } else {
      LAS unsigned char* g1 = tb + (16 + lr) * TROW;
      f32x4 c1 = {0, 0, 0, 0};
#pragma unroll
      for (int s = 0; s < 8; ++s) c1 = mfma_f16(*(LAS v4u*)(g1 + s * 64), *(LAS v4u*)(fb + s * 64), c1);
      c1 *= kscale;
      if (lq < 2) {
        const int base = 256 + 128 * lq + 8 * lr;
        const unsigned d0 = pk_bf16(c1[0], c1[0]), d1 = pk_bf16(c1[1], c1[2]), d2 = pk_bf16(c1[2], c1[2]), d3 = pk_bf16(c1[3], c1[3]);
        if (base != 256) *(LAS u16*)(kx + 2 * (base - 1)) = (u16)d0;
        *(LAS v2u*)(kx + 2 * base) = v2u{d0, d1};
        *(LAS unsigned*)(kx + 2 * (base + 4)) = d2;
        *(LAS u16*)(kx + 2 * (base + 6)) = (u16)d3;
      } else {
#pragma unroll
        for (int e = 0; e < 2; ++e) {
          const int base = 128 * (4 + 2 * (lq - 2) + e) + 8 * lr;
          const unsigned d0 = pk_bf16(c1[2 * e], c1[2 * e]), d1 = pk_bf16(c1[2 * e], c1[2 * e + 1]), d2 = pk_bf16(c1[2 * e + 1], c1[2 * e + 1]);
          if (base != 512) *(LAS unsigned*)(kx + 2 * (base - 2)) = d0;
          else *(LAS u16*)(kx + 2 * (base - 1)) = (u16)d0;
          *(LAS v2u*)(kx + 2 * base) = v2u{d0, d1};
          *(LAS unsigned*)(kx + 2 * (base + 4)) = d2;
        }
      }
    }
  }
  BAR();
  STAMP(4);
  {
    LAS unsigned char* kx = L + B_KX + ch * KX_CH + 32;
    LAS unsigned char* cp = L + ch * CPY_CH;
#pragma unroll
    for (int it = 0; it < 3; ++it) {
      const int a = (it < 2) ? n + 128 * it : 256 + (n & 1);
      if (it < 2 || n < 2) {
        const int L0 = 2048 - 8 * a;
        const v4u lo = *(LAS v4u*)(kx + 2 * (L0 - 8));
        const v2u hi = *(LAS v2u*)(kx + 2 * L0);
        const unsigned E0 = __builtin_amdgcn_alignbit(hi.y, hi.y, 16), E1 = __builtin_amdgcn_alignbit(hi.x, hi.x, 16);
        const unsigned E2 = __builtin_amdgcn_alignbit(lo.w, lo.w, 16), E3 = __builtin_amdgcn_alignbit(lo.z, lo.z, 16);
        const unsigned E4 = __builtin_amdgcn_alignbit(lo.y, lo.y, 16);
        const unsigned O0 = (hi.y & 0xFFFFu) | (hi.x & 0xFFFF0000u), O1 = (hi.x & 0xFFFFu) | (lo.w & 0xFFFF0000u);
        const unsigned O2 = (lo.w & 0xFFFFu) | (lo.z & 0xFFFF0000u), O3 = (lo.z & 0xFFFFu) | (lo.y & 0xFFFF0000u);
        const unsigned O4 = (lo.y & 0xFFFFu) | (lo.x & 0xFFFF0000u);
        *(LAS v4u*)(cp + 3 * CPY + 16 * a) = v4u{E0, E1, E2, E3};
        *(LAS v4u*)(cp + 2 * CPY + 16 * a) = v4u{O0, O1, O2, O3};
        *(LAS v4u*)(cp + 1 * CPY + 16 * a) = v4u{E1, E2, E3, E4};
        *(LAS v4u*)(cp + 0 * CPY + 16 * a) = v4u{O1, O2, O3, O4};
      }
    }
  }
  STAMP(5);
  wait_flags(p.ws, WS_FUT + (g * 32 + (lane & 31)) * FL, lane < 32, tag);
  STAMP(6);
  {
    const int quad = 16 * g + (m >> 1);
    const int off = WS_UT + (quad * 256 + (tid >> 1)) * 64 + ((m & 1) * 2 + (tid & 1)) * 16;
    const v4u a0 = ld_sc1(rs, off), a1 = ld_sc1(rs, off + 128 * 64);
    LAS unsigned char* q = L + B_U + (tid & 1) * U_CH + 512 + (tid >> 1) * 16;
    *(LAS v4u*)q = a0;
    *(LAS v4u*)(q + 2048) = a1;
  }
  BAR();
  STAMP(7);
  f32x4 acc[8];
#pragma unroll
  for (int i = 0; i < 8; ++i) acc[i] = f32x4{0, 0, 0, 0};
  {
    LAS unsigned char* ab = L + ch * CPY_CH + (lr & 3) * CPY + 2 * (2032 - 1792 - 224 + 8 * lq - 4 * (lr >> 2));
    LAS unsigned char* ub = L + B_U + ch * U_CH + 512 + 2 * (16 * lr - 16 + 8 * lq - 224);
    ab += 2 * (224 - 32 * (w & 1));
    ub += 2 * (224 - 32 * (w & 1));
    v4u afA[8], bfA[8], afB[8], bfB[8];
    auto ldc = [&](v4u (&af)[8], v4u (&bf)[8], int rr) {
#pragma unroll
      for (int i = 0; i < 8; ++i) {
        bf[i] = *(LAS v4u*)(ub - 128 * rr + 512 * i);
        v2u x0, x1;
        asm volatile("" ::: "memory");
        x0 = *(LAS v2u*)(ab - 128 * rr + 512 * (7 - i));
        asm volatile("" ::: "memory");
        x1 = *(LAS v2u*)(ab - 128 * rr + 512 * (7 - i) + 8);
        af[i] = v4u{x0.x, x0.y, x1.x, x1.y};
      }
    };
    auto mmc = [&](v4u (&af)[8], v4u (&bf)[8]) {
#pragma unroll
      for (int sm = 0; sm < 8; ++sm)
#pragma unroll
        for (int o = 0; o <= sm; ++o) {
          if (sm + o < 8) acc[sm + o] = mfma_bf16(af[sm], bf[o], acc[sm + o]);
          if (o < sm && sm + o < 8) acc[sm + o] = mfma_bf16(af[o], bf[sm], acc[sm + o]);
        }
    };
    ldc(afA, bfA, 0);
    SB0();
#pragma unroll
    for (int rr = 0; rr < 4; rr += 2) {
      ldc(afB, bfB, rr + 1);
      mmc(afA, bfA);
#pragma unroll
      for (int i = 0; i < 24; ++i) { SGB(0x8, 1); SGB(0x100, 1); }
      SB0();
      if (rr + 2 < 4) ldc(afA, bfA, rr + 2);
      mmc(afB, bfB);
#pragma unroll
      for (int i = 0; i < 24; ++i) { SGB(0x8, 1); SGB(0x100, 1); }
      SB0();
    }
  }
  STAMP(8);
  {
    LAS unsigned char* X = L + 36864 + w * 4096;
    const int keep = (w & 1) * 4;
    f32x4 mine[4];
#pragma unroll
    for (int i = 0; i < 4; ++i) {
      f32x4 lo = acc[i], hi = acc[4 + i];
      asm("" : "+v"(lo), "+v"(hi));
      *(LAS f32x4*)(X + i * 1024 + lane * 16) = (w & 1) ? lo : hi;
      mine[i] = (w & 1) ? hi : lo;
    }
    BAR();
    LAS unsigned char* Y = L + 36864 + (w ^ 1) * 4096;
    const float dk = bf2f(r_dsk);
    v2u yo[4];
#pragma unroll
    for (int i = 0; i < 4; ++i) {
      const f32x4 s = mine[i] + *(LAS f32x4*)(Y + i * 1024 + lane * 16);
      const v2u uu = *(LAS v2u*)(L + B_U + ch * U_CH + 512 + 2 * (256 * (keep + i) + 16 * lr + 4 * lq));
      const unsigned y01 = pk_bf16(s[0], s[1]), y23 = pk_bf16(s[2], s[3]);
      const unsigned k01 = pk_bf16(bf2f(uu.x & 0xFFFFu) * dk, __uint_as_float(uu.x & 0xFFFF0000u) * dk);
      const unsigned k23 = pk_bf16(bf2f(uu.y & 0xFFFFu) * dk, __uint_as_float(uu.y & 0xFFFF0000u) * dk);
      yo[i].x = pk_bf16(bf2f(y01 & 0xFFFFu) + bf2f(k01 & 0xFFFFu), __uint_as_float(y01 & 0xFFFF0000u) + __uint_as_float(k01 & 0xFFFF0000u));
      yo[i].y = pk_bf16(bf2f(y23 & 0xFFFFu) + bf2f(k23 & 0xFFFFu), __uint_as_float(y23 & 0xFFFF0000u) + __uint_as_float(k23 & 0xFFFF0000u));
    }
#pragma unroll
    for (int i = 0; i < 4; i += 2) {
      auto r0 = __builtin_amdgcn_permlane16_swap(yo[i].x, yo[i + 1].x, false, false);
      auto r1 = __builtin_amdgcn_permlane16_swap(yo[i].y, yo[i + 1].y, false, false);
      const int win = keep + i + (lq & 1);
      st_sc1(rs, WS_YT + ((d * 8 + win) * 2 + (lq >> 1)) * 256 + lr * 16, v4u{r0[0], r1[0], r0[1], r1[1]});
    }
    DRAIN();
  }
  BAR();
  if (tid == 0) set_flag(p.ws, WS_FY + (g * 32 + m) * FL, tag);
  STAMP(9);
  wait_flags(p.ws, WS_FY + (g * 32 + (lane & 31)) * FL, lane < 32, tag);
  STAMP(10);
  {
    const int c0 = tid >> 3, hh = (tid >> 2) & 1, l4 = tid & 3;
    const int off = WS_YT + (((64 * g + c0) * 8 + (m >> 2)) * 2 + hh) * 256 + (4 * (m & 3) + l4) * 16;
    const v4u a0 = ld_sc1(rs, off), a1 = ld_sc1(rs, off + 32 * 4096);
    if (m == 0 && tid == 0) set_flag(p.ws, WS_EPB + 256 * g, tag);
    LAS unsigned char* q = L + c0 * 128 + l4 * 32 + hh * 16;
    *(LAS v4u*)q = a0;
    *(LAS v4u*)(q + 4096) = a1;
  }
  BAR();
  {
    LAS unsigned char* q = L + (8 * lq + ((lane >> 2) & 3)) * 128 + 32 * w + 8 * (lane & 3);
    GAS unsigned char* op = (GAS unsigned char*)(p.out + (size_t)(64 * m + 16 * w + lr) * 1024 + 512 + 64 * g + 8 * lq);
#pragma unroll
    for (int it = 0; it < 2; ++it) *(GAS v4u*)(op + it * 64) = tr_read2(q + it * 4096, q + it * 4096 + 512);
  }
  STAMP(11);
  STAMP_END(12);
}

__global__ void __launch_bounds__(256, 2) fused_kernel(Params p) {
  extern __shared__ __attribute__((aligned(16))) unsigned char smem[];
  LAS unsigned char* L = (LAS unsigned char*)smem;
  if (blockIdx.x >= 256) roleA(p, L, blockIdx.x - 256);
  else roleB(p, L, blockIdx.x);
}

extern "C" void kernel_launch(void* const* d_in, const int* in_sizes, int n_in, void* d_out, int out_size, void* d_ws,
                              size_t ws_size, hipStream_t stream) {
  Params p;
  p.x = (const u16*)d_in[0];
  p.wqkv = (const u16*)d_in[1];
  p.wo = (const u16*)d_in[2];
  p.u = (const u16*)d_in[3];
  p.lar = (const u16*)d_in[4];
  p.aim = (const u16*)d_in[5];
  p.cre = (const u16*)d_in[6];
  p.cim = (const u16*)d_in[7];
  p.ldt = (const u16*)d_in[8];
  p.dsk = (const u16*)d_in[9];
  p.out = (u16*)d_out;
  p.ws = (unsigned char*)d_ws;
  (void)hipFuncSetAttribute((const void*)fused_kernel, hipFuncAttributeMaxDynamicSharedMemorySize, LDS_BYTES);
  fused_kernel<<<512, 256, LDS_BYTES, stream>>>(p);
}
```

```cpp
#include <hip/hip_runtime.h>
#include <stdint.h>

#define LAS __attribute__((address_space(3)))
#define GAS __attribute__((address_space(1)))
#define DEV __device__ __forceinline__
typedef unsigned short u16;
typedef unsigned long long u64;
typedef unsigned v4u __attribute__((ext_vector_type(4)));
typedef unsigned v2u __attribute__((ext_vector_type(2)));
typedef short s16x4 __attribute__((ext_vector_type(4)));
typedef __bf16 bf16x8 __attribute__((ext_vector_type(8)));
typedef __bf16 bf16x2 __attribute__((ext_vector_type(2)));
typedef _Float16 f16x2 __attribute__((ext_vector_type(2)));
typedef _Float16 f16x8 __attribute__((ext_vector_type(8)));
typedef float f32x4 __attribute__((ext_vector_type(4)));
typedef float f32x2 __attribute__((ext_vector_type(2)));
typedef GAS u64 gu64;

#define STAMPS 0

constexpr int FL = 128;
constexpr int WS_EPA = 0;
constexpr int WS_EPB = 256;
constexpr int WS_FKV = 4096;
constexpr int WS_FUT = WS_FKV + 256 * FL;
constexpr int WS_FY = WS_FUT + 256 * FL;
constexpr int WS_FO = WS_FY + 256 * FL;
constexpr int KVP_REC = 2304;
constexpr int WS_KVP = WS_FO + 1024 * FL;
constexpr int WS_O = WS_KVP + 256 * KVP_REC;
constexpr int WS_UT = WS_O + 2 * 1024 * 1024;
constexpr int WS_YT = WS_UT + 2 * 1024 * 1024;
constexpr int WS_STAMP = WS_YT + 2 * 1024 * 1024;
constexpr int WS_BYTES = WS_STAMP + 512 * 4 * 16 * 8;
constexpr int LDS_BYTES = 80 * 1024;
constexpr int CODE_BYTES = 27648;
constexpr unsigned SPIN_CAP = 1u << 18;

struct Params {
  const u16 *x, *wqkv, *wo, *u, *lar, *aim, *cre, *cim, *ldt, *dsk;
  u16* out;
  unsigned char* ws;
};

DEV float bf2f(unsigned v) { return __uint_as_float(v << 16); }
DEV unsigned pk_bf16(float a, float b) {
  f32x2 v = {a, b};
  return __builtin_bit_cast(unsigned, __builtin_convertvector(v, bf16x2));
}
DEV unsigned pk_f16(float a, float b) {
  f32x2 v = {a, b};
  return __builtin_bit_cast(unsigned, __builtin_convertvector(v, f16x2));
}
DEV float rbf(float a) { return __uint_as_float(pk_bf16(a, a) << 16); }
DEV f32x4 mfma_bf16(v4u a, v4u b, f32x4 c) {
  return __builtin_amdgcn_mfma_f32_16x16x32_bf16(__builtin_bit_cast(bf16x8, a), __builtin_bit_cast(bf16x8, b), c, 0, 0, 0);
}
DEV f32x4 mfma_f16(v4u a, v4u b, f32x4 c) {
  return __builtin_amdgcn_mfma_f32_16x16x32_f16(__builtin_bit_cast(f16x8, a), __builtin_bit_cast(f16x8, b), c, 0, 0, 0);
}
DEV f32x4 mfma16_bf16(v2u a, v2u b, f32x4 c) {
  return __builtin_amdgcn_mfma_f32_16x16x16bf16_1k(__builtin_bit_cast(s16x4, a), __builtin_bit_cast(s16x4, b), c, 0, 0, 0);
}
DEV v2u tr_read(LAS unsigned char* p) {
  return __builtin_bit_cast(v2u, __builtin_amdgcn_ds_read_tr16_b64_v4i16((LAS s16x4*)p));
}
DEV v4u tr_read2(LAS unsigned char* p0, LAS unsigned char* p1) {
  v2u a = tr_read(p0), b = tr_read(p1);
  return v4u{a.x, a.y, b.x, b.y};
}
DEV void glds16(const void* sbase, unsigned voff, unsigned lds_dst) {
  unsigned keep;
  asm volatile("s_mov_b32 %0, m0\n\ts_mov_b32 m0, %3\n\ts_nop 0\n\tglobal_load_lds_dwordx4 %1, %2\n\ts_mov_b32 m0, %0"
               : "=&s"(keep) : "v"(voff), "s"(sbase), "s"(lds_dst) : "memory");
}
#define DRAIN() asm volatile("s_waitcnt vmcnt(0)" ::: "memory")
#define BAR() do { asm volatile("s_waitcnt lgkmcnt(0)" ::: "memory"); __builtin_amdgcn_s_barrier(); asm volatile("" ::: "memory"); } while (0)
#define RLX_AGENT __ATOMIC_RELAXED, __HIP_MEMORY_SCOPE_AGENT
constexpr int AUX_SC1 = 16;
typedef __amdgpu_buffer_rsrc_t rsrc_t;
DEV v4u ld_sc1(rsrc_t rs, int off) { return __builtin_amdgcn_raw_buffer_load_b128(rs, off, 0, AUX_SC1); }
DEV void st_sc1(rsrc_t rs, int off, v4u v) { __builtin_amdgcn_raw_buffer_store_b128(v, rs, off, 0, AUX_SC1); }
DEV void wait_flags(unsigned char* ws, int off, bool active, u64 tag) {
  gu64* f = (gu64*)(ws + off);
#pragma unroll 1
  for (unsigned spins = 0; spins < SPIN_CAP; ++spins) {
    bool ok = true;
    if (active) ok = __hip_atomic_load(f, RLX_AGENT) == tag;
    if (__all(ok)) break;
    __builtin_amdgcn_s_sleep(1);
  }
  __builtin_amdgcn_fence(__ATOMIC_ACQUIRE, "wavefront");
}
DEV void set_flag(unsigned char* ws, int off, u64 tag) { __hip_atomic_store((gu64*)(ws + off), tag, RLX_AGENT); }

#define STAMP_DECL
#define STAMP(k) do { } while (0)
#define STAMP_END(n) do { } while (0)

DEV float phi(float t) {
  float e = __builtin_amdgcn_exp2f(t * 1.44269504088896341f);
  e = __builtin_amdgcn_fmed3f(e, 0.f, 1.f);
  return (fmaxf(t, 0.f) + e) + 1e-6f;
}
template <int V> struct IC { static constexpr int value = V; };
struct cf { float x, y; };
DEV cf cmul(cf a, cf b) {
  cf r;
  r.x = __builtin_fmaf(-a.y, b.y, a.x * b.x);
  r.y = __builtin_fmaf(a.y, b.x, a.x * b.y);
  asm("" : "+v"(r.x));
  return r;
}
DEV void gld16(v4u& dst, const void* sbase, unsigned voff) {
  asm volatile("global_load_dwordx4 %0, %1, %2" : "=v"(dst) : "v"(voff), "s"(sbase) : "memory");
}
DEV void gld16_sc1(v4u& dst, const void* sbase, unsigned voff) {
  asm volatile("global_load_dwordx4 %0, %1, %2 sc1" : "=v"(dst) : "v"(voff), "s"(sbase) : "memory");
}
template <int N> DEV void vmwait4(v4u (&r)[4]) {
  asm volatile("s_waitcnt vmcnt(%4)" : "+v"(r[0]), "+v"(r[1]), "+v"(r[2]), "+v"(r[3]) : "n"(N) : "memory");
}
template <int N> DEV void vmwait() { asm volatile("s_waitcnt vmcnt(%0)" ::"n"(N) : "memory"); }
#define SB0() __builtin_amdgcn_sched_barrier(0)
#define SGB(mask, n) __builtin_amdgcn_sched_group_barrier(mask, n, 0)

DEV void roleA(const Params& p, LAS unsigned char* L, const int a) {
  const int tid = threadIdx.x, lane = tid & 63, w = __builtin_amdgcn_readfirstlane(tid >> 6);
  const int lr = lane & 15, lq = lane >> 4;
  const int g = a & 7, m = a >> 3;
  const int bi = 4 * (g >> 1) + (m >> 3), bj = 8 * (g & 1) + (m & 7);
  const unsigned Lb = (unsigned)(uintptr_t)L;
  const rsrc_t rs = __builtin_amdgcn_make_buffer_rsrc(p.ws, 0, WS_BYTES, 0x00020000);
  STAMP_DECL
  STAMP(0);
  const int rot = ((m & 7) + 2 * (m >> 3)) & 7;
  const unsigned wvoff = (unsigned)(((16 * w + (lane >> 2)) * 1536 + 32 * bj + 8 * ((lane & 3) ^ ((lane >> 5) << 1))) * 2);
  const unsigned xvoff = (unsigned)((128 * bi + 32 * w + (lane >> 3)) * 1024 + (((lane & 7) ^ (lane >> 3)) * 16));
  auto issue = [&](int c) {
    const int kc = ((c + rot) & 7) * 64;
#pragma unroll
    for (int sg = 0; sg < 2; ++sg)
      glds16(p.wqkv + (size_t)kc * 1536 + 512 * (1 + sg), wvoff, Lb + 49152 + (c % 3) * 8192 + sg * 4096 + w * 1024);
#pragma unroll
    for (int r = 0; r < 4; ++r) glds16(p.x + 8 * r * 512 + kc, xvoff, Lb + (c % 3) * 16384 + w * 4096 + r * 1024);
  };
  auto issue_q = [&](int h) {
#pragma unroll
    for (int c = 0; c < 4; ++c)
      glds16(p.wqkv + (size_t)(((4 * h + c + rot) & 7) * 64) * 1536, wvoff, Lb + ((2 + h) % 3) * 16384 + c * 4096 + w * 1024);
  };
  const u64 tag = __hip_atomic_load((gu64*)(p.ws + WS_EPA), RLX_AGENT) + 1;
  issue(0);
  issue(1);
  SB0();
  STAMP(1);
  LAS unsigned char* tb0 = L + (8 * lq + ((lane >> 2) & 3)) * 64 + 32 * (lq & 1) + 8 * (lane & 3);
  LAS unsigned char* tb1 = L + (8 * lq + ((lane >> 2) & 3)) * 64 + 32 * ((lq & 1) ^ 1) + 8 * (lane & 3);
  LAS unsigned char* xr0 = L + w * 4096 + lr * 128 + ((lq ^ (lr & 7)) * 16);
  LAS unsigned char* xr1 = L + w * 4096 + lr * 128 + (((4 + lq) ^ (lr & 7)) * 16);
  v4u xf[8][4];
  f32x4 ak[2][2], av[2][2];
#pragma unroll
  for (int r = 0; r < 2; ++r)
#pragma unroll
    for (int h = 0; h < 2; ++h) { ak[r][h] = f32x4{0, 0, 0, 0}; av[r][h] = f32x4{0, 0, 0, 0}; }
  auto kv_chunk = [&](auto cc) {
    constexpr int c = decltype(cc)::value;
    constexpr int wo_ = 49152 + (c % 3) * 8192, xo_ = (c % 3) * 16384;
    vmwait<(c < 7) ? 6 : 4>();
    __builtin_amdgcn_s_barrier();
    asm volatile("" ::: "memory");
    v4u wa[4], wb[4];
    xf[c][0] = *(LAS v4u*)(xr0 + xo_);
    wa[0] = tr_read2(tb0 + wo_, tb0 + wo_ + 256);
    wa[1] = tr_read2(tb1 + wo_, tb1 + wo_ + 256);
    xf[c][1] = *(LAS v4u*)(xr0 + xo_ + 2048);
    wa[2] = tr_read2(tb0 + wo_ + 4096, tb0 + wo_ + 4096 + 256);
    wa[3] = tr_read2(tb1 + wo_ + 4096, tb1 + wo_ + 4096 + 256);
    SB0();
    if constexpr (c + 2 < 8) issue(c + 2); else issue_q(c - 6);
    SB0();
    xf[c][2] = *(LAS v4u*)(xr1 + xo_);
    wb[0] = tr_read2(tb0 + wo_ + 2048, tb0 + wo_ + 2048 + 256);
    wb[1] = tr_read2(tb1 + wo_ + 2048, tb1 + wo_ + 2048 + 256);
    xf[c][3] = *(LAS v4u*)(xr1 + xo_ + 2048);
    wb[2] = tr_read2(tb0 + wo_ + 4096 + 2048, tb0 + wo_ + 4096 + 2048 + 256);
    wb[3] = tr_read2(tb1 + wo_ + 4096 + 2048, tb1 + wo_ + 4096 + 2048 + 256);
#pragma unroll
    for (int r = 0; r < 2; ++r) {
      ak[r][0] = mfma_bf16(xf[c][r], wa[0], ak[r][0]);
      ak[r][1] = mfma_bf16(xf[c][r], wa[1], ak[r][1]);
      av[r][0] = mfma_bf16(xf[c][r], wa[2], av[r][0]);
      av[r][1] = mfma_bf16(xf[c][r], wa[3], av[r][1]);
    }
#pragma unroll
    for (int i = 0; i < 5; ++i) { SGB(0x8, 1); SGB(0x100, 2); }
    SB0();
#pragma unroll
    for (int r = 0; r < 2; ++r) {
      ak[r][0] = mfma_bf16(xf[c][2 + r], wb[0], ak[r][0]);
      ak[r][1] = mfma_bf16(xf[c][2 + r], wb[1], ak[r][1]);
      av[r][0] = mfma_bf16(xf[c][2 + r], wb[2], av[r][0]);
      av[r][1] = mfma_bf16(xf[c][2 + r], wb[3], av[r][1]);
    }
    SB0();
  };
  kv_chunk(IC<0>{}); kv_chunk(IC<1>{}); kv_chunk(IC<2>{}); kv_chunk(IC<3>{});
  kv_chunk(IC<4>{}); kv_chunk(IC<5>{}); kv_chunk(IC<6>{}); kv_chunk(IC<7>{});
  STAMP(2);
  LAS unsigned char* S = L + 73728;
  LAS unsigned char* SZ = L + 49152;
  {
    const v4u ones = {0x3F803F80u, 0x3F803F80u, 0x3F803F80u, 0x3F803F80u};
#pragma unroll
    for (int h = 0; h < 2; ++h) {
      v4u pk, vv;
      pk.x = pk_bf16(phi(ak[0][h][0]), phi(ak[0][h][1]));
      pk.y = pk_bf16(phi(ak[0][h][2]), phi(ak[0][h][3]));
      pk.z = pk_bf16(phi(ak[1][h][0]), phi(ak[1][h][1]));
      pk.w = pk_bf16(phi(ak[1][h][2]), phi(ak[1][h][3]));
      vv.x = pk_bf16(av[0][h][0], av[0][h][1]);
      vv.y = pk_bf16(av[0][h][2], av[0][h][3]);
      vv.z = pk_bf16(av[1][h][0], av[1][h][1]);
      vv.w = pk_bf16(av[1][h][2], av[1][h][3]);
      const f32x4 kv = mfma_bf16(pk, vv, f32x4{0, 0, 0, 0});
      const f32x4 zz = mfma_bf16(pk, ones, f32x4{0, 0, 0, 0});
      *(LAS f32x4*)(S + w * 2048 + h * 1024 + lane * 16) = kv;
      if (lr == 0) *(LAS f32x4*)(SZ + w * 128 + h * 64 + lq * 16) = zz;
    }
  }
  asm volatile("" ::"v"(tag));
  DRAIN();
  BAR();
  if (w == 0) {
    const int rec = WS_KVP + (bj * 16 + bi) * KVP_REC;
    f32x4 s0 = *(LAS f32x4*)(S + lane * 16), s1 = *(LAS f32x4*)(S + 1024 + lane * 16);
    f32x4 sz = *(LAS f32x4*)(SZ + (lane & 7) * 16);
#pragma unroll
    for (int ww = 1; ww < 4; ++ww) {
      s0 += *(LAS f32x4*)(S + ww * 2048 + lane * 16);
      s1 += *(LAS f32x4*)(S + ww * 2048 + 1024 + lane * 16);
      sz += *(LAS f32x4*)(SZ + ww * 128 + (lane & 7) * 16);
    }
    st_sc1(rs, rec + lane * 16, __builtin_bit_cast(v4u, s0));
    st_sc1(rs, rec + 1024 + lane * 16, __builtin_bit_cast(v4u, s1));
    if (lane < 8) st_sc1(rs, rec + 2048 + lane * 16, __builtin_bit_cast(v4u, sz));
    DRAIN();
    if (lane == 0) set_flag(p.ws, WS_FKV + (bj * 16 + bi) * FL, tag);
  }
  STAMP(3);
  const int R = 4 * g + (m >> 3), C = m & 7;
  const unsigned ovoff = (unsigned)(((lane >> 3) * 512 + 64 * C + 8 * ((lane & 7) ^ (((lane >> 4) & 3) << 1))) * 2);
#pragma unroll
  for (int n = 0; n < 8; ++n) glds16(p.wo + (size_t)(64 * w + 8 * n) * 512, ovoff, Lb + ((w < 2) ? 16384 + w * 8192 : 57344 + (w - 2) * 8192) + n * 1024);
  f32x4 aq[2][2];
#pragma unroll
  for (int r = 0; r < 2; ++r)
#pragma unroll
    for (int h = 0; h < 2; ++h) aq[r][h] = f32x4{0, 0, 0, 0};
  {
    v4u qa[4], qb[4];
    auto ldq = [&](v4u (&q)[4], int c) {
      const int o = (c < 4) ? 32768 + c * 4096 : (c - 4) * 4096;
      q[0] = tr_read2(tb0 + o, tb0 + o + 256);
      q[1] = tr_read2(tb1 + o, tb1 + o + 256);
      q[2] = tr_read2(tb0 + o + 2048, tb0 + o + 2048 + 256);
      q[3] = tr_read2(tb1 + o + 2048, tb1 + o + 2048 + 256);
    };
    auto mmq = [&](v4u (&q)[4], int c) {
#pragma unroll
      for (int kk = 0; kk < 2; ++kk)
#pragma unroll
        for (int r = 0; r < 2; ++r) {
          aq[r][0] = mfma_bf16(q[2 * kk], xf[c][2 * kk + r], aq[r][0]);
          aq[r][1] = mfma_bf16(q[2 * kk + 1], xf[c][2 * kk + r], aq[r][1]);
        }
    };
    ldq(qa, 0);
    SB0();
#pragma unroll
    for (int c = 0; c < 8; c += 2) {
      ldq(qb, c + 1);
      mmq(qa, c);
#pragma unroll
      for (int i = 0; i < 8; ++i) { SGB(0x8, 1); SGB(0x100, 1); }
      SB0();
      if (c + 2 < 8) ldq(qa, c + 2);
      mmq(qb, c + 1);
#pragma unroll
      for (int i = 0; i < 8; ++i) { SGB(0x8, 1); SGB(0x100, 1); }
      SB0();
    }
  }
  v2u pq[2][2];
#pragma unroll
  for (int r = 0; r < 2; ++r)
#pragma unroll
    for (int h = 0; h < 2; ++h) {
      pq[r][h].x = pk_bf16(phi(aq[r][h][0]), phi(aq[r][h][1]));
      pq[r][h].y = pk_bf16(phi(aq[r][h][2]), phi(aq[r][h][3]));
    }
  BAR();
  STAMP(4);
  wait_flags(p.ws, WS_FKV + (bj * 16 + 4 * w + (lane & 3)) * FL, lane < 4, tag);
  STAMP(5);
  {
    v4u t0[4], t1[4], tz[4];
#pragma unroll
    for (int q = 0; q < 4; ++q) {
      const int rec = WS_KVP + (bj * 16 + 4 * w + q) * KVP_REC;
      gld16_sc1(t0[q], p.ws + rec, lane * 16);
      gld16_sc1(t1[q], p.ws + rec + 1024, lane * 16);
      gld16_sc1(tz[q], p.ws + rec + 2048, (lane & 7) * 16);
    }
    SB0();
#pragma unroll
    for (int n = 0; n < 8; ++n) glds16(p.wo + (size_t)(256 + 64 * w + 8 * n) * 512, ovoff, Lb + ((w < 2) ? w * 8192 : 32768 + (w - 2) * 8192) + n * 1024);
    asm volatile("s_waitcnt vmcnt(8)" : "+v"(t0[0]), "+v"(t0[1]), "+v"(t0[2]), "+v"(t0[3]), "+v"(t1[0]), "+v"(t1[1]), "+v"(t1[2]), "+v"(t1[3]), "+v"(tz[0]), "+v"(tz[1]), "+v"(tz[2]), "+v"(tz[3]));
    f32x4 s0 = __builtin_bit_cast(f32x4, t0[0]), s1 = __builtin_bit_cast(f32x4, t1[0]), sz = __builtin_bit_cast(f32x4, tz[0]);
#pragma unroll
    for (int q = 1; q < 4; ++q) {
      s0 += __builtin_bit_cast(f32x4, t0[q]);
      s1 += __builtin_bit_cast(f32x4, t1[q]);
      sz += __builtin_bit_cast(f32x4, tz[q]);
    }
    *(LAS f32x4*)(S + w * 2048 + lane * 16) = s0;
    *(LAS f32x4*)(S + w * 2048 + 1024 + lane * 16) = s1;
    if (lane < 8) *(LAS f32x4*)(SZ + w * 128 + lane * 16) = sz;
  }
  BAR();
  STAMP(6);
  {
    v2u kvb[2], zb[2];
#pragma unroll
    for (int h = 0; h < 2; ++h) {
      f32x4 s = *(LAS f32x4*)(S + h * 1024 + lane * 16);
      f32x4 z = *(LAS f32x4*)(SZ + h * 64 + lq * 16);
#pragma unroll
      for (int ww = 1; ww < 4; ++ww) {
        s += *(LAS f32x4*)(S + ww * 2048 + h * 1024 + lane * 16);
        z += *(LAS f32x4*)(SZ + ww * 128 + h * 64 + lq * 16);
      }
      kvb[h] = v2u{pk_bf16(s[0], s[1]), pk_bf16(s[2], s[3])};
      zb[h] = v2u{pk_bf16(z[0], z[1]), pk_bf16(z[2], z[3])};
    }
#pragma unroll
    for (int r = 0; r < 2; ++r) {
      v4u ov;
#pragma unroll
      for (int h = 0; h < 2; ++h) {
        const f32x4 num = mfma16_bf16(kvb[h], pq[r][h], f32x4{0, 0, 0, 0});
        const f32x4 den = mfma16_bf16(zb[h], pq[r][h], f32x4{0, 0, 0, 0});
        const float rd = __builtin_amdgcn_rcpf(den[0] + 1e-6f);
        const unsigned lo = pk_bf16(num[0] * rd, num[1] * rd), hi = pk_bf16(num[2] * rd, num[3] * rd);
        if (h == 0) { ov.x = lo; ov.y = hi; } else { ov.z = lo; ov.w = hi; }
      }
      st_sc1(rs, WS_O + ((8 * bi + 2 * w + r) * 16 + bj) * 1024 + lane * 16, ov);
    }
    DRAIN();
    if (lane == 0) set_flag(p.ws, WS_FO + ((bi * 4 + w) * 16 + bj) * FL, tag);
  }
  STAMP(7);
  const int T = 4 * R + w;
  wait_flags(p.ws, WS_FO + ((T >> 1) * 16 + lr) * FL, lane < 16, tag);
  STAMP(8);
  v4u of[16];
#pragma unroll
  for (int j = 0; j < 16; ++j) of[j] = ld_sc1(rs, WS_O + (T * 16 + j) * 1024 + lane * 16);
  if (a == 0 && tid == 0) set_flag(p.ws, WS_EPA, tag);
  __builtin_amdgcn_sched_barrier(0);
  asm volatile("" ::"v"(of[0]));
  BAR();
  STAMP(9);
  f32x4 acc[4];
#pragma unroll
  for (int c = 0; c < 4; ++c) acc[c] = f32x4{0, 0, 0, 0};
  {
    const int ra = 4 * lq + ((lane >> 2) & 3);
    const int sw = (ra >> 1) & 3;
    LAS unsigned char* ob = L + 128 * ra + 8 * (lane & 3);
    v4u fa[4], fb[4];
    auto ldw = [&](v4u (&f)[4], int j) {
      const int off = (j < 4) ? 16384 + j * 4096 : (j < 8) ? 57344 + (j - 4) * 4096 : (j < 12) ? (j - 8) * 4096 : 32768 + (j - 12) * 4096;
#pragma unroll
      for (int c = 0; c < 4; ++c) {
        LAS unsigned char* q = ob + 32 * (c ^ sw) + off;
        f[c] = tr_read2(q, q + 2048);
      }
    };
    ldw(fa, 0);
    SB0();
#pragma unroll
    for (int j = 0; j < 16; j += 2) {
      ldw(fb, j + 1);
#pragma unroll
      for (int c = 0; c < 4; ++c) acc[c] = mfma_bf16(fa[c], of[j], acc[c]);
#pragma unroll
      for (int i = 0; i < 4; ++i) { SGB(0x8, 1); SGB(0x100, 2); }
      SB0();
      if (j + 2 < 16) ldw(fa, j + 2);
#pragma unroll
      for (int c = 0; c < 4; ++c) acc[c] = mfma_bf16(fb[c], of[j + 1], acc[c]);
#pragma unroll
      for (int i = 0; i < 4; ++i) { SGB(0x8, 1); SGB(0x100, 2); }
      SB0();
    }
  }
  STAMP(10);
  {
    GAS unsigned char* op = (GAS unsigned char*)(p.out + (size_t)(16 * T + lr) * 1024 + 64 * C) + (lq & 1) * 32 + (lq >> 1) * 16;
#pragma unroll
    for (int c = 0; c < 4; c += 2) {
      unsigned a0 = pk_bf16(acc[c][0], acc[c][1]), a1 = pk_bf16(acc[c][2], acc[c][3]);
      unsigned b0 = pk_bf16(acc[c + 1][0], acc[c + 1][1]), b1 = pk_bf16(acc[c + 1][2], acc[c + 1][3]);
      auto r0 = __builtin_amdgcn_permlane16_swap(a0, b0, false, false);
      auto r1 = __builtin_amdgcn_permlane16_swap(a1, b1, false, false);
      *(GAS v4u*)(op + c * 32) = v4u{r0[0], r1[0], r0[1], r1[1]};
    }
  }
  STAMP(11);
  STAMP_END(12);
}

constexpr int TROW = 528;
constexpr int TAB_CH = 57 * TROW;
constexpr int B_TAB = 0;
constexpr int B_KX = 60416;
constexpr int KX_CH = 4352;
constexpr int B_U = B_KX + 2 * KX_CH;
constexpr int U_CH = 4608;
constexpr int CPY = 4160;
constexpr int CPY_CH = 4 * CPY;
static_assert(B_U + 2 * U_CH <= LDS_BYTES, "lds");

DEV cf cpow_direct(float al, float be, float pw) {
  const float mag = __builtin_amdgcn_exp2f(al * pw);
  const float ph = __builtin_amdgcn_fractf(be * pw);
  return cf{mag * __builtin_amdgcn_cosf(ph), mag * __builtin_amdgcn_sinf(ph)};
}

DEV void roleB(const Params& p, LAS unsigned char* L, const int b, const unsigned char* code) {
  const int tid = threadIdx.x, lane = tid & 63, w = __builtin_amdgcn_readfirstlane(tid >> 6);
  const int lr = lane & 15, lq = lane >> 4;
  const int g = b & 7, m = b >> 3;
  const int ch = w >> 1;
  const int d = 64 * g + 2 * m + ch;
  const rsrc_t rs = __builtin_amdgcn_make_buffer_rsrc(p.ws, 0, WS_BYTES, 0x00020000);
  STAMP_DECL
  STAMP(0);
  unsigned tcode = 0;
  if (w == 3) tcode = *(const GAS unsigned*)(code + min(((m & 3) * 64 + lane) * 128, CODE_BYTES - 128));
  const GAS v4u* up = (const GAS v4u*)(p.u + (size_t)(64 * m + (tid >> 3)) * 512 + 64 * g + 8 * (tid & 7));
  const v4u ut0 = up[0], ut1 = up[32 * 64];
  const u64 tag = __hip_atomic_load((gu64*)(p.ws + WS_EPB + 256 * g), RLX_AGENT) + 1;
  const int n = tid & 127;
  const unsigned r_lar = p.lar[d * 128 + n], r_aim = p.aim[d * 128 + n];
  const unsigned r_cre = p.cre[d * 128 + n], r_cim = p.cim[d * 128 + n];
  const unsigned r_cre2 = p.cre[d * 128 + (n ^ 64)], r_cim2 = p.cim[d * 128 + (n ^ 64)];
  const unsigned r_ldt = p.ldt[d], r_dsk = p.dsk[d];
  {
    unsigned t;
    if (w == 0) t = *(const GAS unsigned*)(p.wqkv + (size_t)(16 * m + (lane >> 2)) * 1536 + 256 * (g & 1) + 64 * (lane & 3));
    else if (w < 3) t = *(const GAS unsigned*)(p.wo + (size_t)(m * 128 + (w - 1) * 64 + lane) * 64);
    else t = tcode;
    asm volatile("" ::"v"(t));
  }
  *(LAS v4u*)(L + (tid >> 3) * 128 + (tid & 7) * 16) = ut0;
  *(LAS v4u*)(L + 4096 + (tid >> 3) * 128 + (tid & 7) * 16) = ut1;
  if (tid < 64) *(LAS v4u*)(L + B_U + (tid >> 5) * U_CH + (tid & 31) * 16) = v4u{0, 0, 0, 0};
  else if (tid < 68) *(LAS v4u*)(L + B_KX + ((tid >> 1) & 1) * KX_CH + (tid & 1) * 16) = v4u{0, 0, 0, 0};
  BAR();
  STAMP(1);
  {
    LAS unsigned char* q = L + (8 * lq + ((lane >> 2) & 3)) * 128 + 32 * w + 8 * (lane & 3);
#pragma unroll
    for (int it = 0; it < 2; ++it) {
      const v4u v = tr_read2(q + it * 4096, q + it * 4096 + 512);
      const int quad = 16 * g + 4 * w + (lr >> 2), oct = 8 * m + 4 * it + lq;
      st_sc1(rs, WS_UT + (quad * 256 + oct) * 64 + (lr & 3) * 16, v);
    }
    DRAIN();
  }
  BAR();
  if (tid == 0) set_flag(p.ws, WS_FUT + (g * 32 + m) * FL, tag);
  STAMP(2);
  float kscale;
  {
    const float dt = rbf(expf(bf2f(r_ldt)));
    const float ar = -rbf(expf(bf2f(r_lar))) * dt;
    const float al = ar * 1.44269504088896341f;
    const float be = (bf2f(r_aim) * dt) * 0.15915494309189535f;
    unsigned mx = max(max(r_cre & 0x7FFFu, r_cim & 0x7FFFu), max(r_cre2 & 0x7FFFu, r_cim2 & 0x7FFFu));
#pragma unroll
    for (int o = 32; o >= 1; o >>= 1) mx = max(mx, (unsigned)__shfl_xor((int)mx, o));
    int ex = (int)(mx >> 7);
    ex = min(max(ex, 1), 253);
    const float cs = __uint_as_float((unsigned)(254 - ex) << 23);
    kscale = 2.f * dt * __uint_as_float((unsigned)ex << 23);
    const cf z1 = cpow_direct(al, be, 1.f), z8 = cpow_direct(al, be, 8.f), z128 = cpow_direct(al, be, 128.f);
    const cf z1024 = cpow_direct(al, be, 1024.f);
    LAS unsigned char* tp = L + B_TAB + ch * TAB_CH + n * 4;
#define PUTG(row, v) do { const cf v_ = (v); *(LAS unsigned*)(tp + (row) * TROW) = pk_f16(v_.x, v_.y); } while (0)
    cf zp[8];
    zp[0] = cf{1.f, 0.f};
    zp[1] = z1;
#pragma unroll
    for (int i = 2; i < 8; ++i) zp[i] = cmul(zp[i - 1], z1);
#pragma unroll
    for (int i = 0; i < 8; ++i) PUTG(i, zp[i]);
    cf f = cf{bf2f(r_cre) * cs, bf2f(r_cim) * cs};
    cf h = z128, h8 = z1024;
#pragma unroll
    for (int i = 0; i < 16; ++i) {
      *(LAS unsigned*)(tp + (41 + i) * TROW) = pk_f16(f.x, -f.y);
      if (i < 15) f = cmul(f, z8);
      if (i < 9) {
        PUTG(32 + i, h8);
        if (i < 8) h8 = cmul(h8, z128);
      }
      if (i >= 1 && i <= 7) {
        if (i == 1) {
          PUTG(8, h);
#pragma unroll
          for (int q = 1; q < 8; ++q) PUTG(8 + q, cmul(h, zp[q]));
        } else if (i <= 3) {
          PUTG(16 + 4 * (i - 2), h);
#pragma unroll
          for (int q = 1; q < 4; ++q) PUTG(16 + 4 * (i - 2) + q, cmul(h, zp[2 * q]));
        } else {
          PUTG(24 + 2 * (i - 4), h);
          PUTG(24 + 2 * (i - 4) + 1, cmul(h, zp[4]));
        }
        if (i < 7) h = cmul(h, z128);
      }
    }
#undef PUTG
  }
  BAR();
  STAMP(3);
  {
    LAS unsigned char* tb = L + B_TAB + ch * TAB_CH + lq * 16;
    LAS unsigned char* kx = L + B_KX + ch * KX_CH + 32;
    LAS unsigned char* fb = tb + (41 + lr) * TROW;
    if ((w & 1) == 0) {
      LAS unsigned char* g0 = tb + lr * TROW;
      LAS unsigned char* g2 = tb + (32 + min(lr, 8)) * TROW;
      f32x4 c0 = {0, 0, 0, 0}, c2 = {0, 0, 0, 0};
#pragma unroll
      for (int s = 0; s < 8; ++s) {
        const v4u fv = *(LAS v4u*)(fb + s * 64);
        c0 = mfma_f16(*(LAS v4u*)(g0 + s * 64), fv, c0);
        c2 = mfma_f16(*(LAS v4u*)(g2 + s * 64), fv, c2);
      }
      c0 *= kscale;
      c2 *= kscale;
      *(LAS v2u*)(kx + 2 * (128 * (lq >> 1) + 8 * lr + 4 * (lq & 1))) = v2u{pk_bf16(c0[0], c0[1]), pk_bf16(c0[2], c0[3])};
#pragma unroll
      for (int r = 0; r < 4; ++r) {
        const unsigned vv = pk_bf16(c2[r], c2[r]);
        const unsigned pv = (unsigned)__builtin_amdgcn_update_dpp(0, (int)vv, 0x111, 0xf, 0xf, false);
        const unsigned first = (lr & 1) ? ((pv & 0xFFFFu) | (vv & 0xFFFF0000u)) : vv;
        const int pp = 128 * (8 + 4 * lq + r) + 8 * lr;
        if (lq < 2) {
          if (pp != 1024) *(LAS unsigned*)(kx + 2 * (pp - 4)) = first;
          *(LAS unsigned*)(kx + 2 * (pp - 2)) = vv;
          *(LAS v2u*)(kx + 2 * pp) = v2u{vv, vv};
        } else if (pp == 2048) {
          *(LAS v2u*)(kx + 2 * (pp - 4)) = v2u{vv, vv};
        }
      }
    } else {
      LAS unsigned char* g1 = tb + (16 + lr) * TROW;
      f32x4 c1 = {0, 0, 0, 0};
#pragma unroll
      for (int s = 0; s < 8; ++s) c1 = mfma_f16(*(LAS v4u*)(g1 + s * 64), *(LAS v4u*)(fb + s * 64), c1);
      c1 *= kscale;
      if (lq < 2) {
        const int base = 256 + 128 * lq + 8 * lr;
        const unsigned d0 = pk_bf16(c1[0], c1[0]), d1 = pk_bf16(c1[1], c1[2]), d2 = pk_bf16(c1[2], c1[2]), d3 = pk_bf16(c1[3], c1[3]);
        if (base != 256) *(LAS u16*)(kx + 2 * (base - 1)) = (u16)d0;
        *(LAS v2u*)(kx + 2 * base) = v2u{d0, d1};
        *(LAS unsigned*)(kx + 2 * (base + 4)) = d2;
        *(LAS u16*)(kx + 2 * (base + 6)) = (u16)d3;
      } else {
#pragma unroll
        for (int e = 0; e < 2; ++e) {
          const int base = 128 * (4 + 2 * (lq - 2) + e) + 8 * lr;
          const unsigned d0 = pk_bf16(c1[2 * e], c1[2 * e]), d1 = pk_bf16(c1[2 * e], c1[2 * e + 1]), d2 = pk_bf16(c1[2 * e + 1], c1[2 * e + 1]);
          if (base != 512) *(LAS unsigned*)(kx + 2 * (base - 2)) = d0;
          else *(LAS u16*)(kx + 2 * (base - 1)) = (u16)d0;
          *(LAS v2u*)(kx + 2 * base) = v2u{d0, d1};
          *(LAS unsigned*)(kx + 2 * (base + 4)) = d2;
        }
      }
    }
  }
  BAR();
  STAMP(4);
  {
    LAS unsigned char* kx = L + B_KX + ch * KX_CH + 32;
    LAS unsigned char* cp = L + ch * CPY_CH;
#pragma unroll
    for (int it = 0; it < 3; ++it) {
      const int a = (it < 2) ? n + 128 * it : 256 + (n & 1);
      if (it < 2 || n < 2) {
        const int L0 = 2048 - 8 * a;
        const v4u lo = *(LAS v4u*)(kx + 2 * (L0 - 8));
        const v2u hi = *(LAS v2u*)(kx + 2 * L0);
        const unsigned E0 = __builtin_amdgcn_alignbit(hi.y, hi.y, 16), E1 = __builtin_amdgcn_alignbit(hi.x, hi.x, 16);
        const unsigned E2 = __builtin_amdgcn_alignbit(lo.w, lo.w, 16), E3 = __builtin_amdgcn_alignbit(lo.z, lo.z, 16);
        const unsigned E4 = __builtin_amdgcn_alignbit(lo.y, lo.y, 16);
        const unsigned O0 = (hi.y & 0xFFFFu) | (hi.x & 0xFFFF0000u), O1 = (hi.x & 0xFFFFu) | (lo.w & 0xFFFF0000u);
        const unsigned O2 = (lo.w & 0xFFFFu) | (lo.z & 0xFFFF0000u), O3 = (lo.z & 0xFFFFu) | (lo.y & 0xFFFF0000u);
        const unsigned O4 = (lo.y & 0xFFFFu) | (lo.x & 0xFFFF0000u);
        *(LAS v4u*)(cp + 3 * CPY + 16 * a) = v4u{E0, E1, E2, E3};
        *(LAS v4u*)(cp + 2 * CPY + 16 * a) = v4u{O0, O1, O2, O3};
        *(LAS v4u*)(cp + 1 * CPY + 16 * a) = v4u{E1, E2, E3, E4};
        *(LAS v4u*)(cp + 0 * CPY + 16 * a) = v4u{O1, O2, O3, O4};
      }
    }
  }
  STAMP(5);
  wait_flags(p.ws, WS_FUT + (g * 32 + (lane & 31)) * FL, lane < 32, tag);
  STAMP(6);
  {
    const int quad = 16 * g + (m >> 1);
    const int off = WS_UT + (quad * 256 + (tid >> 1)) * 64 + ((m & 1) * 2 + (tid & 1)) * 16;
    const v4u a0 = ld_sc1(rs, off), a1 = ld_sc1(rs, off + 128 * 64);
    LAS unsigned char* q = L + B_U + (tid & 1) * U_CH + 512 + (tid >> 1) * 16;
    *(LAS v4u*)q = a0;
    *(LAS v4u*)(q + 2048) = a1;
  }
  BAR();
  STAMP(7);
  f32x4 acc[8];
#pragma unroll
  for (int i = 0; i < 8; ++i) acc[i] = f32x4{0, 0, 0, 0};
  {
    LAS unsigned char* ab = L + ch * CPY_CH + (lr & 3) * CPY + 2 * (2032 - 1792 - 224 + 8 * lq - 4 * (lr >> 2));
    LAS unsigned char* ub = L + B_U + ch * U_CH + 512 + 2 * (16 * lr - 16 + 8 * lq - 224);
    ab += 2 * (224 - 32 * (w & 1));
    ub += 2 * (224 - 32 * (w & 1));
    v4u afA[8], bfA[8], afB[8], bfB[8];
    auto ldc = [&](v4u (&af)[8], v4u (&bf)[8], int rr) {
#pragma unroll
      for (int i = 0; i < 8; ++i) {
        bf[i] = *(LAS v4u*)(ub - 128 * rr + 512 * i);
        v2u x0, x1;
        asm volatile("" ::: "memory");
        x0 = *(LAS v2u*)(ab - 128 * rr + 512 * (7 - i));
        asm volatile("" ::: "memory");
        x1 = *(LAS v2u*)(ab - 128 * rr + 512 * (7 - i) + 8);
        af[i] = v4u{x0.x, x0.y, x1.x, x1.y};
      }
    };
    auto mmc = [&](v4u (&af)[8], v4u (&bf)[8]) {
#pragma unroll
      for (int sm = 0; sm < 8; ++sm)
#pragma unroll
        for (int o = 0; o <= sm; ++o) {
          if (sm + o < 8) acc[sm + o] = mfma_bf16(af[sm], bf[o], acc[sm + o]);
          if (o < sm && sm + o < 8) acc[sm + o] = mfma_bf16(af[o], bf[sm], acc[sm + o]);
        }
    };
    ldc(afA, bfA, 0);
    SB0();
#pragma unroll
    for (int rr = 0; rr < 4; rr += 2) {
      ldc(afB, bfB, rr + 1);
      mmc(afA, bfA);
#pragma unroll
      for (int i = 0; i < 24; ++i) { SGB(0x8, 1); SGB(0x100, 1); }
      SB0();
      if (rr + 2 < 4) ldc(afA, bfA, rr + 2);
      mmc(afB, bfB);
#pragma unroll
      for (int i = 0; i < 24; ++i) { SGB(0x8, 1); SGB(0x100, 1); }
      SB0();
    }
  }
  STAMP(8);
  {
    LAS unsigned char* X = L + 36864 + w * 4096;
    const int keep = (w & 1) * 4;
    f32x4 mine[4];
#pragma unroll
    for (int i = 0; i < 4; ++i) {
      f32x4 lo = acc[i], hi = acc[4 + i];
      asm("" : "+v"(lo), "+v"(hi));
      *(LAS f32x4*)(X + i * 1024 + lane * 16) = (w & 1) ? lo : hi;
      mine[i] = (w & 1) ? hi : lo;
    }
    BAR();
    LAS unsigned char* Y = L + 36864 + (w ^ 1) * 4096;
    const float dk = bf2f(r_dsk);
    v2u yo[4];
#pragma unroll
    for (int i = 0; i < 4; ++i) {
      const f32x4 s = mine[i] + *(LAS f32x4*)(Y + i * 1024 + lane * 16);
      const v2u uu = *(LAS v2u*)(L + B_U + ch * U_CH + 512 + 2 * (256 * (keep + i) + 16 * lr + 4 * lq));
      const unsigned y01 = pk_bf16(s[0], s[1]), y23 = pk_bf16(s[2], s[3]);
      const unsigned k01 = pk_bf16(bf2f(uu.x & 0xFFFFu) * dk, __uint_as_float(uu.x & 0xFFFF0000u) * dk);
      const unsigned k23 = pk_bf16(bf2f(uu.y & 0xFFFFu) * dk, __uint_as_float(uu.y & 0xFFFF0000u) * dk);
      yo[i].x = pk_bf16(bf2f(y01 & 0xFFFFu) + bf2f(k01 & 0xFFFFu), __uint_as_float(y01 & 0xFFFF0000u) + __uint_as_float(k01 & 0xFFFF0000u));
      yo[i].y = pk_bf16(bf2f(y23 & 0xFFFFu) + bf2f(k23 & 0xFFFFu), __uint_as_float(y23 & 0xFFFF0000u) + __uint_as_float(k23 & 0xFFFF0000u));
    }
#pragma unroll
    for (int i = 0; i < 4; i += 2) {
      auto r0 = __builtin_amdgcn_permlane16_swap(yo[i].x, yo[i + 1].x, false, false);
      auto r1 = __builtin_amdgcn_permlane16_swap(yo[i].y, yo[i + 1].y, false, false);
      const int win = keep + i + (lq & 1);
      st_sc1(rs, WS_YT + ((d * 8 + win) * 2 + (lq >> 1)) * 256 + lr * 16, v4u{r0[0], r1[0], r0[1], r1[1]});
    }
    DRAIN();
  }
  BAR();
  if (tid == 0) set_flag(p.ws, WS_FY + (g * 32 + m) * FL, tag);
  STAMP(9);
  wait_flags(p.ws, WS_FY + (g * 32 + (lane & 31)) * FL, lane < 32, tag);
  STAMP(10);
  {
    const int c0 = tid >> 3, hh = (tid >> 2) & 1, l4 = tid & 3;
    const int off = WS_YT + (((64 * g + c0) * 8 + (m >> 2)) * 2 + hh) * 256 + (4 * (m & 3) + l4) * 16;
    const v4u a0 = ld_sc1(rs, off), a1 = ld_sc1(rs, off + 32 * 4096);
    if (m == 0 && tid == 0) set_flag(p.ws, WS_EPB + 256 * g, tag);
    LAS unsigned char* q = L + c0 * 128 + l4 * 32 + hh * 16;
    *(LAS v4u*)q = a0;
    *(LAS v4u*)(q + 4096) = a1;
  }
  BAR();
  {
    LAS unsigned char* q = L + (8 * lq + ((lane >> 2) & 3)) * 128 + 32 * w + 8 * (lane & 3);
    GAS unsigned char* op = (GAS unsigned char*)(p.out + (size_t)(64 * m + 16 * w + lr) * 1024 + 512 + 64 * g + 8 * lq);
#pragma unroll
    for (int it = 0; it < 2; ++it) *(GAS v4u*)(op + it * 64) = tr_read2(q + it * 4096, q + it * 4096 + 512);
  }
  STAMP(11);
  STAMP_END(12);
}

__global__ void __launch_bounds__(256, 2) fused_kernel(Params p) {
  extern __shared__ __attribute__((aligned(16))) unsigned char smem[];
  LAS unsigned char* L = (LAS unsigned char*)smem;
  const unsigned char* code = (const unsigned char*)(__builtin_amdgcn_s_getpc() & ~(u64)255);
  if (blockIdx.x >= 256) roleA(p, L, blockIdx.x - 256);
  else roleB(p, L, blockIdx.x, code);
}

extern "C" void kernel_launch(void* const* d_in, const int* in_sizes, int n_in, void* d_out, int out_size, void* d_ws,
                              size_t ws_size, hipStream_t stream) {
  Params p;
  p.x = (const u16*)d_in[0];
  p.wqkv = (const u16*)d_in[1];
  p.wo = (const u16*)d_in[2];
  p.u = (const u16*)d_in[3];
  p.lar = (const u16*)d_in[4];
  p.aim = (const u16*)d_in[5];
  p.cre = (const u16*)d_in[6];
  p.cim = (const u16*)d_in[7];
  p.ldt = (const u16*)d_in[8];
  p.dsk = (const u16*)d_in[9];
  p.out = (u16*)d_out;
  p.ws = (unsigned char*)d_ws;
  (void)hipFuncSetAttribute((const void*)fused_kernel, hipFuncAttributeMaxDynamicSharedMemorySize, LDS_BYTES);
  fused_kernel<<<512, 256, LDS_BYTES, stream>>>(p);
}
```

```cpp
#include <hip/hip_runtime.h>
#include <stdint.h>

#if defined(__HIP_DEVICE_COMPILE__)
#pragma clang attribute push(__attribute__((target("no-packed-fp32-ops"))), apply_to = function)
#endif
#define LAS __attribute__((address_space(3)))
#define GAS __attribute__((address_space(1)))
#define DEV __device__ __forceinline__
typedef unsigned short u16;
typedef unsigned long long u64;
typedef unsigned v4u __attribute__((ext_vector_type(4)));
typedef unsigned v2u __attribute__((ext_vector_type(2)));
typedef short s16x4 __attribute__((ext_vector_type(4)));
typedef __bf16 bf16x8 __attribute__((ext_vector_type(8)));
typedef __bf16 bf16x2 __attribute__((ext_vector_type(2)));
typedef _Float16 f16x2 __attribute__((ext_vector_type(2)));
typedef _Float16 f16x8 __attribute__((ext_vector_type(8)));
typedef float f32x4 __attribute__((ext_vector_type(4)));
typedef float f32x2 __attribute__((ext_vector_type(2)));
typedef GAS u64 gu64;

#define STAMPS 0

#define GATHER_AT 4
constexpr int WS_EPA = 0;
constexpr int WS_EPB = 256;
constexpr int WS_FA = 4096;
constexpr int WS_F1 = WS_FA + 256 * 128;
constexpr int WS_F2 = WS_F1 + 256 * 128;
constexpr int KVP_REC = 3072;
constexpr int WS_KVP = WS_F2 + 256 * 128;
constexpr int WS_O = WS_KVP + 256 * KVP_REC;
constexpr int WS_UT = WS_O + 2 * 1024 * 1024;
constexpr int WS_YT = WS_UT + 2 * 1024 * 1024;
constexpr int WS_STAMP = WS_YT + 2 * 1024 * 1024;
constexpr int WS_BYTES = WS_STAMP + 512 * 4 * 16 * 8;
constexpr int LDS_BYTES = 80 * 1024;
constexpr int CODE_BYTES = 24320;
constexpr unsigned SPIN_CAP = 1u << 16;

struct Params {
  const u16 *x, *wqkv, *wo, *u, *lar, *aim, *cre, *cim, *ldt, *dsk;
  u16* out;
  unsigned char* ws;
};

DEV float u2f(unsigned v) { return __builtin_bit_cast(float, v); }
DEV bool wany(bool c) { return __builtin_amdgcn_ballot_w64(c) != 0; }
DEV bool wall(bool c) { return __builtin_amdgcn_ballot_w64(!c) == 0; }
DEV float bf2f(unsigned v) { return u2f(v << 16); }
DEV unsigned pk_bf16(float a, float b) {
  f32x2 v = {a, b};
  return __builtin_bit_cast(unsigned, __builtin_convertvector(v, bf16x2));
}
DEV unsigned pk_f16(float a, float b) {
  f32x2 v = {a, b};
  return __builtin_bit_cast(unsigned, __builtin_convertvector(v, f16x2));
}
DEV float rbf(float a) { return u2f(pk_bf16(a, a) << 16); }
DEV f32x4 mfma_bf16(v4u a, v4u b, f32x4 c) {
  return __builtin_amdgcn_mfma_f32_16x16x32_bf16(__builtin_bit_cast(bf16x8, a), __builtin_bit_cast(bf16x8, b), c, 0, 0, 0);
}
DEV f32x4 mfma_f16(v4u a, v4u b, f32x4 c) {
  return __builtin_amdgcn_mfma_f32_16x16x32_f16(__builtin_bit_cast(f16x8, a), __builtin_bit_cast(f16x8, b), c, 0, 0, 0);
}
DEV f32x4 mfma16_bf16(v2u a, v2u b, f32x4 c) {
  return __builtin_amdgcn_mfma_f32_16x16x16bf16_1k(__builtin_bit_cast(s16x4, a), __builtin_bit_cast(s16x4, b), c, 0, 0, 0);
}
DEV v2u tr_read(LAS unsigned char* p) {
  return __builtin_bit_cast(v2u, __builtin_amdgcn_ds_read_tr16_b64_v4i16((LAS s16x4*)p));
}
DEV v4u tr_read2(LAS unsigned char* p0, LAS unsigned char* p1) {
  v2u a = tr_read(p0), b = tr_read(p1);
  return v4u{a.x, a.y, b.x, b.y};
}
#define DRAIN() asm volatile("s_waitcnt vmcnt(0)" ::: "memory")
#define BAR() do { asm volatile("s_waitcnt lgkmcnt(0)" ::: "memory"); __builtin_amdgcn_s_barrier(); asm volatile("" ::: "memory"); } while (0)
#define RLX_AGENT __ATOMIC_RELAXED, __HIP_MEMORY_SCOPE_AGENT
constexpr int AUX_SC1 = 16;
typedef __amdgpu_buffer_rsrc_t rsrc_t;
DEV void set_flag(unsigned char* ws, int off, u64 tag) { __hip_atomic_store((gu64*)(ws + off), tag, RLX_AGENT); }

#define STAMP_ARGS
#define STAMP_PASS
#define STAMP_DECL
#define STAMP(k) do { } while (0)
#define STAMP_END(n) do { } while (0)

constexpr float PHI_EPS = 1e-6f;
template <int N> DEV void phi_n(float (&u)[N]) {
  float e[N];
#pragma unroll
  for (int i = 0; i < N; ++i) e[i] = __builtin_fmaf(u[i], 1.44269504088896341f, -1.44269504088896341e-6f);
#pragma unroll
  for (int i = 0; i < N; ++i) e[i] = __builtin_amdgcn_fmed3f(__builtin_amdgcn_exp2f(e[i]), 0.f, 1.f);
#pragma unroll
  for (int i = 0; i < N; ++i) asm("v_max_f32 %0, %1, %0" : "+v"(u[i]) : "s"(PHI_EPS));
#pragma unroll
  for (int i = 0; i < N; ++i) u[i] += e[i];
}
template <int V> struct IC { static constexpr int value = V; };
struct cf { float x, y; };
DEV cf cmul(cf a, cf b) { return cf{__builtin_fmaf(-a.y, b.y, a.x * b.x), __builtin_fmaf(a.y, b.x, a.x * b.y)}; }
DEV cf csq(cf a) { return cf{__builtin_fmaf(-a.y, a.y, a.x * a.x), (a.x + a.x) * a.y}; }
DEV unsigned nosent(unsigned v) { return min(v, 0xFFFFFFFEu); }
DEV v4u nosent4(v4u v) { return v4u{nosent(v.x), nosent(v.y), nosent(v.z), nosent(v.w)}; }
DEV unsigned umax3(unsigned a, unsigned b, unsigned c) { return max(max(a, b), c); }
DEV bool has_sent(v4u v) { return umax3(umax3(v.x, v.y, v.z), v.w, 0u) == 0xFFFFFFFFu; }
#define NTF " nt"
DEV rsrc_t mk_rsrc(const void* p, int bytes) { return __builtin_amdgcn_make_buffer_rsrc((void*)p, 0, bytes, 0x00020000); }
DEV void dma1(unsigned m0v, unsigned voff, rsrc_t rs, unsigned soff) {
  asm volatile("s_mov_b32 m0, %0\n\ts_nop 0\n\tbuffer_load_dwordx4 %1, %2, %3 offen lds" ::"s"(m0v), "v"(voff), "s"(rs), "s"(soff) : "memory");
}
DEV void dma2(unsigned m0v, unsigned voff, rsrc_t rs, unsigned soff) {
  asm volatile("s_mov_b32 m0, %0\n\ts_nop 0\n\tbuffer_load_dwordx4 %1, %2, %3 offen lds\n\tbuffer_load_dwordx4 %1, %2, %3 offen offset:1024 lds"
               ::"s"(m0v), "v"(voff), "s"(rs), "s"(soff) : "memory");
}
DEV void dma4nt(unsigned m0v, unsigned voff, rsrc_t rs, unsigned soff) {
  asm volatile("s_mov_b32 m0, %0\n\ts_nop 0\n\tbuffer_load_dwordx4 %1, %2, %3 offen" NTF " lds\n\tbuffer_load_dwordx4 %1, %2, %3 offen offset:1024" NTF " lds\n\t"
               "buffer_load_dwordx4 %1, %2, %3 offen offset:2048" NTF " lds\n\tbuffer_load_dwordx4 %1, %2, %3 offen offset:3072" NTF " lds"
               ::"s"(m0v), "v"(voff), "s"(rs), "s"(soff) : "memory");
}
template <int IMM> DEV void dma1nt(unsigned m0v, unsigned voff, rsrc_t rs, unsigned soff) {
  asm volatile("s_mov_b32 m0, %0\n\ts_nop 0\n\tbuffer_load_dwordx4 %1, %2, %3 offen offset:%4" NTF " lds" ::"s"(m0v), "v"(voff), "s"(rs), "s"(soff), "n"(IMM) : "memory");
}
template <int IMM> DEV void dma1i(unsigned m0v, unsigned voff, rsrc_t rs, unsigned soff) {
  asm volatile("s_mov_b32 m0, %0\n\ts_nop 0\n\tbuffer_load_dwordx4 %1, %2, %3 offen offset:%4 lds" ::"s"(m0v), "v"(voff), "s"(rs), "s"(soff), "n"(IMM) : "memory");
}
DEV void dma4v(unsigned m0v, const unsigned (&voff)[4], rsrc_t rs, unsigned soff) {
  asm volatile("s_mov_b32 m0, %0\n\ts_nop 0\n\tbuffer_load_dwordx4 %1, %5, %6 offen lds\n\tbuffer_load_dwordx4 %2, %5, %6 offen offset:1024 lds\n\t"
               "buffer_load_dwordx4 %3, %5, %6 offen offset:2048 lds\n\tbuffer_load_dwordx4 %4, %5, %6 offen offset:3072 lds"
               ::"s"(m0v), "v"(voff[0]), "v"(voff[1]), "v"(voff[2]), "v"(voff[3]), "s"(rs), "s"(soff) : "memory");
}
template <int IMM> DEV void bst16(v4u v, rsrc_t rs, unsigned voff, unsigned soff) {
  asm volatile("buffer_store_dwordx4 %0, %1, %2, %3 offen offset:%4 sc1\n\ts_nop 1" ::"v"(v), "v"(voff), "s"(rs), "s"(soff), "n"(IMM) : "memory");
}
template <int IMM> DEV void bld16(v4u& d, rsrc_t rs, unsigned voff, unsigned soff) {
  asm volatile("buffer_load_dwordx4 %0, %1, %2, %3 offen offset:%4 sc1" : "=v"(d) : "v"(voff), "s"(rs), "s"(soff), "n"(IMM) : "memory");
}
template <int IMM> DEV void bld16_re(v4u& d, rsrc_t rs, unsigned voff, unsigned soff) {
  asm volatile("buffer_load_dwordx4 %0, %1, %2, %3 offen offset:%4 sc1" ::"v"(d), "v"(voff), "s"(rs), "s"(soff), "n"(IMM) : "memory");
}
DEV void bld8(v2u& d, rsrc_t rs, unsigned voff, unsigned soff) {
  asm volatile("buffer_load_dwordx2 %0, %1, %2, %3 offen sc1" : "=v"(d) : "v"(voff), "s"(rs), "s"(soff) : "memory");
}
template <int N> DEV void vmwait() { asm volatile("s_waitcnt vmcnt(%0)" ::"n"(N) : "memory"); }
#define SB0() __builtin_amdgcn_sched_barrier(0)
#define SGB(mask, n) __builtin_amdgcn_sched_group_barrier(mask, n, 0)

#define A_SETUP() \
  const int tid = threadIdx.x, lane = tid & 63, w = __builtin_amdgcn_readfirstlane(tid >> 6); \
  const int g = a & 7, m = a >> 3; \
  const int bi = 4 * (g >> 1) + (m >> 3), bj = 8 * (g & 1) + (m & 7); \
  const unsigned Lb = (unsigned)(uintptr_t)L; \
  const rsrc_t rx = mk_rsrc(p.x, 2048 * 512 * 2), rw = mk_rsrc(p.wqkv, 512 * 1536 * 2); \
  const int rot = ((m & 7) + 2 * (m >> 3)) & 7; \
  const unsigned wvoff = (unsigned)(((16 * w + (lane >> 2)) * 1536 + 32 * bj + 8 * ((lane & 3) ^ ((lane >> 5) << 1))) * 2); \
  const unsigned xvoff = (unsigned)((128 * bi + 32 * w + 4 * (lane >> 3)) * 1024 + (((lane & 7) ^ (lane >> 3)) * 16));
DEV void roleA_first(const Params& p, LAS unsigned char* L, const int a) {
  A_SETUP()
#pragma unroll
  for (int c = 0; c < 2; ++c) {
    const unsigned kc = (unsigned)((c + rot) & 7);
    dma2(Lb + 49152 + c * 8192 + w * 2048, wvoff, rw, kc * (64 * 3072) + 1024);
    dma4nt(Lb + c * 16384 + w * 4096, xvoff, rx, kc * 128);
  }
}
#define APRIO 2
DEV void roleA(const Params& p, LAS unsigned char* L, const int a STAMP_ARGS) {
  A_SETUP()
  const int lr = lane & 15, lq = lane >> 4;
  const int R = 2 * bi + (bj >> 3), C = bj & 7;
  __builtin_amdgcn_s_setprio(APRIO);
  const rsrc_t ro = mk_rsrc(p.wo, 512 * 512 * 2), rs = mk_rsrc(p.ws, WS_BYTES);
  STAMP(1);
  const unsigned l16 = lane * 16;
  const unsigned o_dst = WS_O + ((8 * bi + 2 * w) * 16 + bj) * 1024;
  const unsigned kv_dst = WS_KVP + (bj * 16 + bi) * KVP_REC + min(w, 2) * 1024;
  const int tbo = (8 * (lq & 1) + ((lane >> 2) & 3)) * 64 + 8 * (lane & 3);
  LAS unsigned char* tb0 = L + (lq >> 1) * 2048 + tbo + 32 * (lq & 1);
  LAS unsigned char* tb1 = L + (lq >> 1) * 2048 + tbo + 32 * ((lq & 1) ^ 1);
  LAS unsigned char* xr0 = L + w * 4096 + lr * 128 + ((lq ^ (lr & 7)) * 16);
  LAS unsigned char* xr1 = L + w * 4096 + lr * 128 + (((4 + lq) ^ (lr & 7)) * 16);
  const unsigned fa_voff = (unsigned)((bi * 16 + 4 * w + (lane & 3)) * 128);
  v2u ep, fl;
  v4u xf[8][4];
  f32x4 ak[2][2], av[2][2];
#pragma unroll
  for (int r = 0; r < 2; ++r)
#pragma unroll
    for (int h = 0; h < 2; ++h) { ak[r][h] = f32x4{PHI_EPS, PHI_EPS, PHI_EPS, PHI_EPS}; av[r][h] = f32x4{0, 0, 0, 0}; }
  auto kv_chunk = [&](auto cc) {
    constexpr int c = decltype(cc)::value;
    constexpr int wo_ = 49152 + (c % 3) * 8192, xo_ = (c % 3) * 16384;
    vmwait<(c < 7) ? 6 : 5>();
    __builtin_amdgcn_s_barrier();
    asm volatile("" ::: "memory");
    v4u wa[4], wb[4];
    xf[c][0] = *(LAS v4u*)(xr0 + xo_);
    wa[0] = tr_read2(tb0 + wo_, tb0 + wo_ + 256);
    wa[1] = tr_read2(tb1 + wo_, tb1 + wo_ + 256);
    xf[c][1] = *(LAS v4u*)(xr0 + xo_ + 2048);
    wa[2] = tr_read2(tb0 + wo_ + 1024, tb0 + wo_ + 1024 + 256);
    wa[3] = tr_read2(tb1 + wo_ + 1024, tb1 + wo_ + 1024 + 256);
    SB0();
    if constexpr (c == 6) {
      bld8(ep, rs, 0u, WS_EPA);
    }
    const unsigned kc = (unsigned)((c + 2 + rot) & 7);
    auto piece = [&](auto ee) {
      constexpr int e = decltype(ee)::value;
      if constexpr (c + 2 < 8) {
        if constexpr (e < 2) dma1i<e * 1024>(Lb + 49152 + ((c + 2) % 3) * 8192 + w * 2048, wvoff, rw, kc * (64 * 3072) + 1024);
        else dma1nt<(e - 2) * 1024>(Lb + ((c + 2) % 3) * 16384 + w * 4096, xvoff, rx, kc * 128);
      } else if constexpr (e < 4) {
        dma1(Lb + ((c - 4) % 3) * 16384 + e * 4096 + w * 1024, wvoff, rw, (unsigned)((4 * (c - 6) + e + rot) & 7) * (64 * 3072));
      }
      SB0();
    };
    piece(IC<0>{});
    xf[c][2] = *(LAS v4u*)(xr1 + xo_);
    wb[0] = tr_read2(tb0 + wo_ + 4096, tb0 + wo_ + 4096 + 256);
    wb[1] = tr_read2(tb1 + wo_ + 4096, tb1 + wo_ + 4096 + 256);
    ak[0][0] = mfma_bf16(xf[c][0], wa[0], ak[0][0]);
    ak[0][1] = mfma_bf16(xf[c][0], wa[1], ak[0][1]);
    SB0();
    piece(IC<1>{});
    xf[c][3] = *(LAS v4u*)(xr1 + xo_ + 2048);
    wb[2] = tr_read2(tb0 + wo_ + 5120, tb0 + wo_ + 5120 + 256);
    wb[3] = tr_read2(tb1 + wo_ + 5120, tb1 + wo_ + 5120 + 256);
    ak[1][0] = mfma_bf16(xf[c][1], wa[0], ak[1][0]);
    ak[1][1] = mfma_bf16(xf[c][1], wa[1], ak[1][1]);
    av[0][0] = mfma_bf16(xf[c][0], wa[2], av[0][0]);
    SB0();
    piece(IC<2>{});
    av[0][1] = mfma_bf16(xf[c][0], wa[3], av[0][1]);
    av[1][0] = mfma_bf16(xf[c][1], wa[2], av[1][0]);
    av[1][1] = mfma_bf16(xf[c][1], wa[3], av[1][1]);
    SB0();
    piece(IC<3>{});
    ak[0][0] = mfma_bf16(xf[c][2], wb[0], ak[0][0]);
    ak[0][1] = mfma_bf16(xf[c][2], wb[1], ak[0][1]);
    ak[1][0] = mfma_bf16(xf[c][3], wb[0], ak[1][0]);
    SB0();
    piece(IC<4>{});
    ak[1][1] = mfma_bf16(xf[c][3], wb[1], ak[1][1]);
    av[0][0] = mfma_bf16(xf[c][2], wb[2], av[0][0]);
    av[0][1] = mfma_bf16(xf[c][2], wb[3], av[0][1]);
    SB0();
    piece(IC<5>{});
    av[1][0] = mfma_bf16(xf[c][3], wb[2], av[1][0]);
    av[1][1] = mfma_bf16(xf[c][3], wb[3], av[1][1]);
    SB0();
  };
  kv_chunk(IC<0>{}); kv_chunk(IC<1>{}); kv_chunk(IC<2>{}); kv_chunk(IC<3>{});
  kv_chunk(IC<4>{}); kv_chunk(IC<5>{}); kv_chunk(IC<6>{}); kv_chunk(IC<7>{});
  STAMP(2);
  {
    const v4u sent = {0xFFFFFFFFu, 0xFFFFFFFFu, 0xFFFFFFFFu, 0xFFFFFFFFu};
    bst16<0>(sent, rs, l16, o_dst);
    bst16<0>(sent, rs, l16, o_dst + 16384);
  }
  LAS unsigned char* S = L + 73728;
  LAS unsigned char* SZ = L + 49152;
  {
    const v4u ones = {0x3F803F80u, 0x3F803F80u, 0x3F803F80u, 0x3F803F80u};
    float t[16];
#pragma unroll
    for (int i = 0; i < 16; ++i) t[i] = ak[(i >> 2) & 1][i >> 3][i & 3];
    phi_n<16>(t);
    f32x4 kv[2], zz[2];
#pragma unroll
    for (int h = 0; h < 2; ++h) {
      const v4u pk = {pk_bf16(t[8 * h], t[8 * h + 1]), pk_bf16(t[8 * h + 2], t[8 * h + 3]), pk_bf16(t[8 * h + 4], t[8 * h + 5]), pk_bf16(t[8 * h + 6], t[8 * h + 7])};
      const v4u vv = {pk_bf16(av[0][h][0], av[0][h][1]), pk_bf16(av[0][h][2], av[0][h][3]), pk_bf16(av[1][h][0], av[1][h][1]), pk_bf16(av[1][h][2], av[1][h][3])};
      kv[h] = mfma_bf16(pk, vv, f32x4{0, 0, 0, 0});
      zz[h] = mfma_bf16(ones, pk, f32x4{0, 0, 0, 0});
    }
    *(LAS f32x4*)(S + w * 2048 + l16) = kv[0];
    *(LAS f32x4*)(S + w * 2048 + 1024 + l16) = f32x4{kv[0][3], kv[1][0], kv[1][1], 0.f};
    *(LAS f32x4*)(SZ + w * 2048 + l16) = f32x4{kv[1][2], kv[1][3], (lq & 1) ? zz[1][0] : zz[0][0], 0.f};
  }
  LAS unsigned char* psrc = ((w < 2) ? S + w * 1024 : SZ) + l16;
  asm volatile("s_waitcnt vmcnt(10)" : "+v"(ep)::"memory");
  const u64 tag = (((u64)ep.y << 32) | ep.x) + 1;
  const unsigned tag32 = (unsigned)tag;
  BAR();
  {
    const f32x4 s0 = *(LAS f32x4*)(psrc), s1 = *(LAS f32x4*)(psrc + 2048), s2 = *(LAS f32x4*)(psrc + 4096), s3 = *(LAS f32x4*)(psrc + 6144);
    v4u o = __builtin_bit_cast(v4u, (s0 + s1) + (s2 + s3));
    o.w = tag32;
    bst16<0>(o, rs, l16, kv_dst);
  }
  STAMP(3);
  vmwait<3>();
  BAR();
  unsigned ovoff[4];
#pragma unroll
  for (int r = 0; r < 4; ++r) ovoff[r] = (unsigned)(4 * (lane >> 3) * 1024 + 128 * C + 16 * ((lane & 7) ^ (r << 1)));
  f32x4 aq[2][2];
#pragma unroll
  for (int r = 0; r < 2; ++r)
#pragma unroll
    for (int h = 0; h < 2; ++h) aq[r][h] = f32x4{PHI_EPS, PHI_EPS, PHI_EPS, PHI_EPS};
  v4u t0[4], t1[4], tz[4];
  auto gather = [&]() {
#pragma unroll
    for (int q = 0; q < 4; ++q) {
      const unsigned rec = WS_KVP + (bj * 16 + 4 * w + q) * KVP_REC;
      bld16<0>(t0[q], rs, l16, rec);
      bld16<1024>(t1[q], rs, l16, rec);
      bld16<2048>(tz[q], rs, l16, rec);
    }
  };
  auto gather_bad = [&]() {
    unsigned x = 0;
#pragma unroll
    for (int q = 0; q < 4; ++q) x |= (t0[q].w ^ tag32) | (t1[q].w ^ tag32) | (tz[q].w ^ tag32);
    return wany(x != 0);
  };
#define GATHER_WAIT() asm volatile("s_waitcnt vmcnt(0)" : "+v"(t0[0]), "+v"(t0[1]), "+v"(t0[2]), "+v"(t0[3]), "+v"(t1[0]), "+v"(t1[1]), "+v"(t1[2]), "+v"(t1[3]), "+v"(tz[0]), "+v"(tz[1]), "+v"(tz[2]), "+v"(tz[3])::"memory")
  {
    LAS unsigned char* qb0 = tb0 - (lq >> 1) * 1024;
    LAS unsigned char* qb1 = tb1 - (lq >> 1) * 1024;
    v4u qa[4], qb[4];
    auto ldq = [&](v4u (&q)[4], int c) {
      const int o = (c < 4) ? 32768 + c * 4096 : (c - 4) * 4096;
      q[0] = tr_read2(qb0 + o, qb0 + o + 256);
      q[1] = tr_read2(qb1 + o, qb1 + o + 256);
      q[2] = tr_read2(qb0 + o + 2048, qb0 + o + 2048 + 256);
      q[3] = tr_read2(qb1 + o + 2048, qb1 + o + 2048 + 256);
    };
    auto mmq = [&](v4u (&q)[4], int c) {
#pragma unroll
      for (int kk = 0; kk < 2; ++kk)
#pragma unroll
        for (int r = 0; r < 2; ++r) {
          aq[r][0] = mfma_bf16(q[2 * kk], xf[c][2 * kk + r], aq[r][0]);
          aq[r][1] = mfma_bf16(q[2 * kk + 1], xf[c][2 * kk + r], aq[r][1]);
        }
    };
    ldq(qa, 0);
    SB0();
#pragma unroll
    for (int c = 0; c < 8; c += 2) {
      if (c == 2) {
        vmwait<4>();
        __builtin_amdgcn_s_barrier();
        asm volatile("" ::: "memory");
        if (tid == 0) set_flag(p.ws, WS_FA + (bi * 16 + bj) * 128, tag);
        SB0();
      }
      if (c < 4) { dma4v(Lb + (c ? 57344 : 16384) + w * 4096, ovoff, ro, (unsigned)(2 * c + w) * 32768); SB0(); }
      ldq(qb, c + 1);
      mmq(qa, c);
#pragma unroll
      for (int i = 0; i < 8; ++i) { SGB(0x8, 1); SGB(0x100, 1); }
      SB0();
      if (c + 2 < 8) ldq(qa, c + 2);
      mmq(qb, c + 1);
#pragma unroll
      for (int i = 0; i < 8; ++i) { SGB(0x8, 1); SGB(0x100, 1); }
      SB0();
      if (c == GATHER_AT) { gather(); SB0(); }
    }
  }
  v2u pq[2][2];
  {
    float t[16];
#pragma unroll
    for (int i = 0; i < 16; ++i) t[i] = aq[i >> 3][(i >> 2) & 1][i & 3];
    phi_n<16>(t);
#pragma unroll
    for (int i = 0; i < 4; ++i) pq[i >> 1][i & 1] = v2u{pk_bf16(t[4 * i], t[4 * i + 1]), pk_bf16(t[4 * i + 2], t[4 * i + 3])};
  }
  asm volatile("" : "+v"(pq[0][0]), "+v"(pq[0][1]), "+v"(pq[1][0]), "+v"(pq[1][1]));
  STAMP(4);
  {
    unsigned spins = 0;
    GATHER_WAIT();
    while (gather_bad() && ++spins < SPIN_CAP) {
      __builtin_amdgcn_s_sleep(4);
      gather();
      GATHER_WAIT();
    }
  }
  STAMP(5);
  {
    auto sum4 = [](const v4u (&t)[4], int e) { return (u2f(t[0][e]) + u2f(t[1][e])) + (u2f(t[2][e]) + u2f(t[3][e])); };
    *(LAS f32x4*)(S + w * 2048 + l16) = f32x4{sum4(t0, 0), sum4(t0, 1), sum4(t0, 2), sum4(t1, 0)};
    *(LAS f32x4*)(S + w * 2048 + 1024 + l16) = f32x4{sum4(t1, 1), sum4(t1, 2), sum4(tz, 0), sum4(tz, 1)};
    *(LAS float*)(SZ + w * 256 + lane * 4) = sum4(tz, 2);
    bld8(fl, rs, fa_voff, WS_FA);
  }
  BAR();
  STAMP(6);
  {
    f32x4 rs_[2][4], rz_[2][4];
#pragma unroll
    for (int h = 0; h < 2; ++h)
#pragma unroll
      for (int ww = 0; ww < 4; ++ww) {
        rs_[h][ww] = *(LAS f32x4*)(S + ww * 2048 + h * 1024 + l16);
        rz_[h][ww] = *(LAS f32x4*)(SZ + ww * 256 + h * 64 + lq * 16);
      }
    SB0();
    f32x4 num[2][2], den[2][2];
#pragma unroll
    for (int h = 0; h < 2; ++h) {
      const f32x4 s = (rs_[h][0] + rs_[h][1]) + (rs_[h][2] + rs_[h][3]);
      const f32x4 z = (rz_[h][0] + rz_[h][1]) + (rz_[h][2] + rz_[h][3]);
      const v2u kvb = v2u{pk_bf16(s[0], s[1]), pk_bf16(s[2], s[3])};
      const v2u zb = v2u{pk_bf16(z[0], z[1]), pk_bf16(z[2], z[3])};
#pragma unroll
      for (int r = 0; r < 2; ++r) {
        den[r][h] = mfma16_bf16(zb, pq[r][h], f32x4{1e-6f, 1e-6f, 1e-6f, 1e-6f});
        num[r][h] = mfma16_bf16(kvb, pq[r][h], f32x4{0, 0, 0, 0});
      }
    }
#pragma unroll
    for (int r = 0; r < 2; ++r) {
      const float r0 = __builtin_amdgcn_rcpf(den[r][0][0]), r1 = __builtin_amdgcn_rcpf(den[r][1][0]);
      const v4u ov = {pk_bf16(num[r][0][0] * r0, num[r][0][1] * r0), pk_bf16(num[r][0][2] * r0, num[r][0][3] * r0),
                      pk_bf16(num[r][1][0] * r1, num[r][1][1] * r1), pk_bf16(num[r][1][2] * r1, num[r][1][3] * r1)};
      bst16<0>(nosent4(ov), rs, l16, o_dst + r * 16384);
    }
  }
  STAMP(7);
  dma4v(Lb + w * 4096, ovoff, ro, (unsigned)(8 + w) * 32768);
  dma4v(Lb + 32768 + w * 4096, ovoff, ro, (unsigned)(12 + w) * 32768);
  const int T = 4 * R + w;
  const unsigned o_src = WS_O + T * 16384;
  const int ra = (lane >> 2) & 3;
  LAS unsigned char* ob = L + ra * 1024 + lq * 128 + 8 * (lane & 3);
  auto ldw = [&](v4u (&f)[4], int j) {
    const int off = (j < 4) ? 16384 + j * 4096 : (j < 8) ? 57344 + (j - 4) * 4096 : (j < 12) ? (j - 8) * 4096 : 32768 + (j - 12) * 4096;
#pragma unroll
    for (int c = 0; c < 4; ++c) {
      LAS unsigned char* q = ob + 32 * (c ^ ra) + off;
      f[c] = tr_read2(q, q + 512);
    }
  };
  v4u wf[8][4];
#pragma unroll
  for (int j = 0; j < 8; ++j) ldw(wf[j], j);
  asm volatile("s_waitcnt vmcnt(0)" : "+v"(fl)::"memory");
  {
    unsigned spins = 0;
    while (!wall((((u64)fl.y << 32) | fl.x) == tag) && ++spins < SPIN_CAP) {
      __builtin_amdgcn_s_sleep(8);
      bld8(fl, rs, fa_voff, WS_FA);
      asm volatile("s_waitcnt vmcnt(0)" : "+v"(fl)::"memory");
    }
  }
  BAR();
  STAMP(8);
  v4u of[16];
  f32x4 acc[4];
  {
    auto ldo = [&](auto jj) { constexpr int j = decltype(jj)::value; bld16<(j & 3) * 1024>(of[j], rs, l16, o_src + (j >> 2) * 4096); };
    ldo(IC<0>{}); ldo(IC<1>{}); ldo(IC<2>{}); ldo(IC<3>{}); ldo(IC<4>{}); ldo(IC<5>{}); ldo(IC<6>{}); ldo(IC<7>{});
    ldo(IC<8>{}); ldo(IC<9>{}); ldo(IC<10>{}); ldo(IC<11>{}); ldo(IC<12>{}); ldo(IC<13>{}); ldo(IC<14>{}); ldo(IC<15>{});
    STAMP(9);
    unsigned spins = 0;
    for (;;) {
      unsigned bad = 0;
#pragma unroll
      for (int c = 0; c < 4; ++c) acc[c] = f32x4{0, 0, 0, 0};
      auto step = [&](auto jj, v4u (&f)[4]) {
        constexpr int j = decltype(jj)::value;
        asm volatile("s_waitcnt vmcnt(%1)" : "+v"(of[j]) : "n"(15 - j) : "memory");
#pragma unroll
        for (int c = 0; c < 4; ++c) acc[c] = mfma_bf16(f[c], of[j], acc[c]);
        bad |= wany(has_sent(of[j])) ? (1u << j) : 0u;
        SB0();
      };
      v4u fa[4], fb[4];
      step(IC<0>{}, wf[0]); step(IC<1>{}, wf[1]); step(IC<2>{}, wf[2]); step(IC<3>{}, wf[3]);
      step(IC<4>{}, wf[4]); step(IC<5>{}, wf[5]);
      ldw(fa, 8); step(IC<6>{}, wf[6]);
      ldw(fb, 9); step(IC<7>{}, wf[7]);
      step(IC<8>{}, fa); ldw(fa, 10); step(IC<9>{}, fb); ldw(fb, 11);
      step(IC<10>{}, fa); ldw(fa, 12); step(IC<11>{}, fb); ldw(fb, 13);
      step(IC<12>{}, fa); ldw(fa, 14); step(IC<13>{}, fb); ldw(fb, 15);
      step(IC<14>{}, fa); step(IC<15>{}, fb);
      if (bad == 0 || ++spins >= SPIN_CAP) break;
      auto re = [&](auto jj) { constexpr int j = decltype(jj)::value; if (bad & (1u << j)) bld16_re<(j & 3) * 1024>(of[j], rs, l16, o_src + (j >> 2) * 4096); };
      re(IC<0>{}); re(IC<1>{}); re(IC<2>{}); re(IC<3>{}); re(IC<4>{}); re(IC<5>{}); re(IC<6>{}); re(IC<7>{});
      re(IC<8>{}); re(IC<9>{}); re(IC<10>{}); re(IC<11>{}); re(IC<12>{}); re(IC<13>{}); re(IC<14>{}); re(IC<15>{});
      vmwait<0>();
    }
  }
  if (a == 0 && tid == 0) set_flag(p.ws, WS_EPA, tag);
  STAMP(10);
  {
    const int row = 32 * (T >> 1) + 4 * (lr & 7) + 2 * (T & 1) + (lr >> 3);
    GAS unsigned char* op = (GAS unsigned char*)(p.out + (size_t)row * 1024 + 64 * C) + (lq & 1) * 32 + (lq >> 1) * 16;
#pragma unroll
    for (int c = 0; c < 4; c += 2) {
      unsigned a0 = pk_bf16(acc[c][0], acc[c][1]), a1 = pk_bf16(acc[c][2], acc[c][3]);
      unsigned b0 = pk_bf16(acc[c + 1][0], acc[c + 1][1]), b1 = pk_bf16(acc[c + 1][2], acc[c + 1][3]);
      auto r0 = __builtin_amdgcn_permlane16_swap(a0, b0, false, false);
      auto r1 = __builtin_amdgcn_permlane16_swap(a1, b1, false, false);
      *(GAS v4u*)(op + c * 32) = v4u{r0[0], r1[0], r0[1], r1[1]};
    }
  }
  STAMP(11);
  STAMP_END(12);
}

#define FLAG_AT 3
constexpr int TQ = 57 * 16;
constexpr int TAB_CH = 32 * TQ;
constexpr int B_KX = 2 * TAB_CH;
constexpr int KX_CH = 4352;
constexpr int B_U = B_KX + 2 * KX_CH;
constexpr int U_CH = 4608;
constexpr int CPY = 4160;
constexpr int CPY_CH = 4 * CPY;
static_assert(B_U + 2 * U_CH <= LDS_BYTES, "lds");

DEV cf cpow_direct(float al, float be, float pw) {
  const float mag = __builtin_amdgcn_exp2f(al * pw);
  const float ph = __builtin_amdgcn_fractf(be * pw);
  cf r = cf{mag * __builtin_amdgcn_cosf(ph), mag * __builtin_amdgcn_sinf(ph)};
  return r;
}
template <int CTRL, int RM> DEV unsigned dpp_max(unsigned v) {
  return max(v, (unsigned)__builtin_amdgcn_update_dpp((int)v, (int)v, CTRL, RM, 0xf, false));
}

struct BIn { v4u ut0, ut1; unsigned lar, aim, cre, cim, cre2, cim2, ldt, dsk, warm; u64 ep; };
DEV void roleB_first(const Params& p, const int b, const unsigned char* code, BIn& in) {
  const int tid = threadIdx.x;
  const int g = b & 7, m = b >> 3;
  const int d = 64 * g + 2 * m + (tid >> 7);
  const GAS v4u* up = (const GAS v4u*)(p.u + (size_t)(64 * m + (tid >> 3)) * 512 + 64 * g + 8 * (tid & 7));
  in.ut0 = __builtin_nontemporal_load(up);
  in.ut1 = __builtin_nontemporal_load(up + 32 * 64);
  const int e = (64 * g + 2 * m) * 128 + tid;
  in.lar = __builtin_nontemporal_load(p.lar + e);
  in.aim = __builtin_nontemporal_load(p.aim + e);
  in.cre = __builtin_nontemporal_load(p.cre + e);
  in.cim = __builtin_nontemporal_load(p.cim + e);
  in.cre2 = __builtin_nontemporal_load(p.cre + (e ^ 64));
  in.cim2 = __builtin_nontemporal_load(p.cim + (e ^ 64));
  in.ldt = p.ldt[d];
  in.dsk = p.dsk[d];
  in.ep = __hip_atomic_load((gu64*)(p.ws + WS_EPB + 256 * g), RLX_AGENT);
  in.warm = *(const GAS unsigned*)(code + min((m * 8 + (tid & 7)) * 128, CODE_BYTES - 128));
}
DEV void roleB(const Params& p, LAS unsigned char* L, const int b, const BIn& in STAMP_ARGS) {
  const int tid = threadIdx.x, lane = tid & 63, w = __builtin_amdgcn_readfirstlane(tid >> 6);
  const int lr = lane & 15, lq = lane >> 4;
  const int g = b & 7, m = b >> 3;
  const int ch = w >> 1;
  const int d = 64 * g + 2 * m + ch;
  const rsrc_t rs = mk_rsrc(p.ws, WS_BYTES);
  const unsigned l16 = lane * 16;
  const int n = tid & 127;
  const v4u ut0 = in.ut0, ut1 = in.ut1;
  const unsigned r_lar = in.lar, r_aim = in.aim, r_cre = in.cre, r_cim = in.cim, r_cre2 = in.cre2, r_cim2 = in.cim2, r_ldt = in.ldt, r_dsk = in.dsk;
  const unsigned warm = in.warm;
  const u64 tag = in.ep + 1;
  *(LAS v4u*)(L + (tid >> 3) * 128 + (tid & 7) * 16) = ut0;
  *(LAS v4u*)(L + 4096 + (tid >> 3) * 128 + (tid & 7) * 16) = ut1;
  if (w < 2) *(LAS v4u*)(L + ((w == 0) ? B_U + (lane >> 5) * U_CH + (lane & 31) * 16 : B_KX + ((lane >> 1) & 1) * KX_CH + (lane & 1) * 16)) = v4u{0, 0, 0, 0};
  BAR();
  STAMP(1);
  {
    LAS unsigned char* q = L + (8 * lq + ((lane >> 2) & 3)) * 128 + 32 * w + 8 * (lane & 3);
    const v4u v0 = tr_read2(q, q + 512), v1 = tr_read2(q + 4096, q + 4096 + 512);
    const unsigned dst = WS_UT + ((16 * g + 4 * w) * 256 + 8 * m) * 64;
    const unsigned vo = (unsigned)((lr >> 2) * 16384 + lq * 64 + (lr & 3) * 16);
    asm volatile("" ::"v"(r_lar), "v"(r_aim), "v"(r_cre), "v"(r_cim), "v"(r_cre2), "v"(r_cim2), "v"(r_ldt), "v"(r_dsk), "v"(tag), "v"(warm));
    __builtin_amdgcn_s_setprio(3);
    bst16<0>(v0, rs, vo, dst);
    bst16<256>(v1, rs, vo, dst);
    __builtin_amdgcn_s_setprio(0);
  }
  STAMP(2);
  float kscale;
  {
    const float dt = rbf(expf(bf2f(r_ldt)));
    const float ar = -rbf(expf(bf2f(r_lar))) * dt;
    const float al = ar * 1.44269504088896341f;
    const float be = (bf2f(r_aim) * dt) * 0.15915494309189535f;
    unsigned mx = max(max(r_cre & 0x7FFFu, r_cim & 0x7FFFu), max(r_cre2 & 0x7FFFu, r_cim2 & 0x7FFFu));
    mx = dpp_max<0x111, 0xf>(mx); mx = dpp_max<0x112, 0xf>(mx); mx = dpp_max<0x114, 0xf>(mx); mx = dpp_max<0x118, 0xf>(mx);
    mx = dpp_max<0x142, 0xa>(mx); mx = dpp_max<0x143, 0xc>(mx);
    int ex = __builtin_amdgcn_readlane((int)mx, 63) >> 7;
    ex = min(max(ex, 1), 253);
    const float cs = u2f((unsigned)(254 - ex) << 23);
    kscale = 2.f * dt * u2f((unsigned)ex << 23);
    const cf z1 = cpow_direct(al, be, 1.f), z8 = cpow_direct(al, be, 8.f), z128 = cpow_direct(al, be, 128.f);
    unsigned tv[57];
#define PUT(row, v) do { const cf v_ = (v); tv[row] = pk_f16(v_.x, v_.y); } while (0)
    cf zp[8];
    zp[1] = z1; zp[2] = csq(z1); zp[3] = cmul(zp[2], z1); zp[4] = csq(zp[2]);
    zp[5] = cmul(zp[4], z1); zp[6] = csq(zp[3]); zp[7] = cmul(zp[6], z1);
    tv[0] = 0x00003C00u;
#pragma unroll
    for (int i = 1; i < 8; ++i) { PUT(i, zp[i]); PUT(8 + i, cmul(zp[i], z128)); }
    PUT(8, z128);
    asm volatile("" : "+v"(tv[1]), "+v"(tv[2]), "+v"(tv[3]), "+v"(tv[4]), "+v"(tv[5]), "+v"(tv[6]), "+v"(tv[7]), "+v"(tv[8]), "+v"(tv[9]), "+v"(tv[10]), "+v"(tv[11]), "+v"(tv[12]), "+v"(tv[13]), "+v"(tv[14]), "+v"(tv[15]));
    DRAIN();
    BAR();
    __builtin_amdgcn_s_setprio(3);
    if (tid == 0) set_flag(p.ws, WS_F1 + (g * 32 + m) * 128, tag);
    __builtin_amdgcn_s_setprio(0);
    LAS unsigned char* tp = L + ch * TAB_CH + (n >> 2) * TQ + (n & 3) * 4;
#define WR(row) *(LAS unsigned*)(tp + (row) * 16) = tv[row]
#pragma unroll
    for (int i = 0; i < 16; ++i) WR(i);
    cf H[17];
    H[1] = z128;
#pragma unroll
    for (int i = 2; i <= 16; ++i) H[i] = (i & 1) ? cmul(H[i - 1], z128) : csq(H[i / 2]);
#pragma unroll
    for (int i = 2; i < 4; ++i) {
      PUT(16 + 4 * (i - 2), H[i]);
#pragma unroll
      for (int q = 1; q < 4; ++q) PUT(16 + 4 * (i - 2) + q, cmul(H[i], zp[2 * q]));
    }
#pragma unroll
    for (int i = 4; i < 8; ++i) { PUT(24 + 2 * (i - 4), H[i]); PUT(25 + 2 * (i - 4), cmul(H[i], zp[4])); }
#pragma unroll
    for (int i = 8; i <= 16; ++i) PUT(24 + i, H[i]);
#pragma unroll
    for (int i = 16; i < 40; ++i) WR(i);
    const cf z8c = cf{z8.x, -z8.y}, z16c = csq(z8c);
    cf f0 = cf{bf2f(r_cre) * cs, -(bf2f(r_cim) * cs)};
    cf f1 = cmul(f0, z8c);
#pragma unroll
    for (int i = 0; i < 8; ++i) {
      PUT(41 + 2 * i, f0);
      PUT(42 + 2 * i, f1);
      if (i < 7) { f0 = cmul(f0, z16c); f1 = cmul(f1, z16c); }
    }
#pragma unroll
    for (int i = 40; i < 57; ++i) WR(i);
#undef PUT
#undef WR
  }
  BAR();
  STAMP(3);
  {
    LAS unsigned char* tb = L + ch * TAB_CH + lq * TQ;
    LAS unsigned char* kx = L + B_KX + ch * KX_CH + 32;
    LAS unsigned char* fb = tb + (41 + lr) * 16;
    if ((w & 1) == 0) {
      {
        const v4u sent = {0xFFFFFFFFu, 0xFFFFFFFFu, 0xFFFFFFFFu, 0xFFFFFFFFu};
        bst16<0>(sent, rs, l16, WS_YT + d * 4096);
        bst16<1024>(sent, rs, l16, WS_YT + d * 4096);
        bst16<2048>(sent, rs, l16, WS_YT + d * 4096);
        bst16<3072>(sent, rs, l16, WS_YT + d * 4096);
      }
      LAS unsigned char* g0 = tb + lr * 16;
      LAS unsigned char* g2 = tb + (32 + min(lr, 8)) * 16;
      f32x4 c0 = {0, 0, 0, 0}, c2 = {0, 0, 0, 0};
      v4u fv[8], a0[8], a2[8];
#pragma unroll
      for (int s = 0; s < 8; ++s) {
        fv[s] = *(LAS v4u*)(fb + s * 4 * TQ);
        a0[s] = *(LAS v4u*)(g0 + s * 4 * TQ);
        a2[s] = *(LAS v4u*)(g2 + s * 4 * TQ);
      }
      SB0();
#pragma unroll
      for (int s = 0; s < 8; ++s) {
        c0 = mfma_f16(a0[s], fv[s], c0);
        c2 = mfma_f16(a2[s], fv[s], c2);
      }
      c0 *= kscale;
      c2 *= kscale;
      if (lane == 0) c0[0] += bf2f(r_dsk);
      *(LAS v2u*)(kx + 2 * (128 * (lq >> 1) + 8 * lr + 4 * (lq & 1))) = v2u{pk_bf16(c0[0], c0[1]), pk_bf16(c0[2], c0[3])};
#pragma unroll
      for (int r = 0; r < 4; ++r) {
        const unsigned vv = pk_bf16(c2[r], c2[r]);
        const unsigned pv = (unsigned)__builtin_amdgcn_update_dpp(0, (int)vv, 0x111, 0xf, 0xf, false);
        const unsigned first = (lr & 1) ? ((pv & 0xFFFFu) | (vv & 0xFFFF0000u)) : vv;
        const int pp = 128 * (8 + 4 * lq + r) + 8 * lr;
        if (lq < 2) {
          if (pp != 1024) *(LAS unsigned*)(kx + 2 * (pp - 4)) = first;
          *(LAS unsigned*)(kx + 2 * (pp - 2)) = vv;
          *(LAS v2u*)(kx + 2 * pp) = v2u{vv, vv};
        } else if (pp == 2048) {
          *(LAS v2u*)(kx + 2 * (pp - 4)) = v2u{vv, vv};
        }
      }
    } else {
      v2u fl;
      const unsigned fvo = (unsigned)((w >> 1) * 16 + lr) * 128;
      __builtin_amdgcn_s_setprio(3);
      bld8(fl, rs, fvo, WS_F1 + g * 4096);
      __builtin_amdgcn_s_setprio(0);
      LAS unsigned char* g1 = tb + (16 + lr) * 16;
      f32x4 c1 = {0, 0, 0, 0};
      v4u fv[8], a1[8];
#pragma unroll
      for (int s = 0; s < 8; ++s) {
        fv[s] = *(LAS v4u*)(fb + s * 4 * TQ);
        a1[s] = *(LAS v4u*)(g1 + s * 4 * TQ);
      }
      SB0();
#pragma unroll
      for (int s = 0; s < 8; ++s) c1 = mfma_f16(a1[s], fv[s], c1);
      c1 *= kscale;
      if (lq < 2) {
        const int base = 256 + 128 * lq + 8 * lr;
        const unsigned d0 = pk_bf16(c1[0], c1[0]), d1 = pk_bf16(c1[1], c1[2]), d2 = pk_bf16(c1[2], c1[2]), d3 = pk_bf16(c1[3], c1[3]);
        if (base != 256) *(LAS u16*)(kx + 2 * (base - 1)) = (u16)d0;
        *(LAS v2u*)(kx + 2 * base) = v2u{d0, d1};
        *(LAS unsigned*)(kx + 2 * (base + 4)) = d2;
        *(LAS u16*)(kx + 2 * (base + 6)) = (u16)d3;
      } else {
#pragma unroll
        for (int e = 0; e < 2; ++e) {
          const int base = 128 * (4 + 2 * (lq - 2) + e) + 8 * lr;
          const unsigned d0 = pk_bf16(c1[2 * e], c1[2 * e]), d1 = pk_bf16(c1[2 * e], c1[2 * e + 1]), d2 = pk_bf16(c1[2 * e + 1], c1[2 * e + 1]);
          if (base != 512) *(LAS unsigned*)(kx + 2 * (base - 2)) = d0;
          else *(LAS u16*)(kx + 2 * (base - 1)) = (u16)d0;
          *(LAS v2u*)(kx + 2 * base) = v2u{d0, d1};
          *(LAS unsigned*)(kx + 2 * (base + 4)) = d2;
        }
      }
      {
        unsigned spins = 0;
        asm volatile("s_waitcnt vmcnt(0)" : "+v"(fl)::"memory");
        while (!wall((((u64)fl.y << 32) | fl.x) == tag) && ++spins < SPIN_CAP) {
          __builtin_amdgcn_s_sleep(8);
          bld8(fl, rs, fvo, WS_F1 + g * 4096);
          asm volatile("s_waitcnt vmcnt(0)" : "+v"(fl)::"memory");
        }
      }
    }
  }
  BAR();
  v4u ua, ub_;
  {
    const unsigned src = WS_UT + (16 * g + (m >> 1)) * 16384 + (m & 1) * 32;
    const unsigned vo = (unsigned)((tid >> 1) * 64 + (tid & 1) * 16);
    __builtin_amdgcn_s_setprio(3);
    bld16<0>(ua, rs, vo, src);
    bld16<0>(ub_, rs, vo, src + 8192);
    __builtin_amdgcn_s_setprio(0);
  }
  STAMP(4);
  {
    LAS unsigned char* kx = L + B_KX + ch * KX_CH + 32;
    LAS unsigned char* cp = L + ch * CPY_CH;
    v4u lo[3];
    v2u hi[3];
#pragma unroll
    for (int it = 0; it < 3; ++it) {
      const int a = (it < 2) ? n + 128 * it : 256 + (n & 1);
      const int L0 = 2048 - 8 * a;
      lo[it] = *(LAS v4u*)(kx + 2 * (L0 - 8));
      hi[it] = *(LAS v2u*)(kx + 2 * L0);
    }
    SB0();
#pragma unroll
    for (int it = 0; it < 3; ++it) {
      const int a = (it < 2) ? n + 128 * it : 256 + (n & 1);
      if (it < 2 || n < 2) {
        const v4u l = lo[it];
        const v2u hh = hi[it];
        const unsigned E0 = __builtin_amdgcn_alignbit(hh.y, hh.y, 16), E1 = __builtin_amdgcn_alignbit(hh.x, hh.x, 16);
        const unsigned E2 = __builtin_amdgcn_alignbit(l.w, l.w, 16), E3 = __builtin_amdgcn_alignbit(l.z, l.z, 16);
        const unsigned E4 = __builtin_amdgcn_alignbit(l.y, l.y, 16);
        const unsigned O0 = (hh.y & 0xFFFFu) | (hh.x & 0xFFFF0000u), O1 = (hh.x & 0xFFFFu) | (l.w & 0xFFFF0000u);
        const unsigned O2 = (l.w & 0xFFFFu) | (l.z & 0xFFFF0000u), O3 = (l.z & 0xFFFFu) | (l.y & 0xFFFF0000u);
        const unsigned O4 = (l.y & 0xFFFFu) | (l.x & 0xFFFF0000u);
        *(LAS v4u*)(cp + 3 * CPY + 16 * a) = v4u{E0, E1, E2, E3};
        *(LAS v4u*)(cp + 2 * CPY + 16 * a) = v4u{O0, O1, O2, O3};
        *(LAS v4u*)(cp + 1 * CPY + 16 * a) = v4u{E1, E2, E3, E4};
        *(LAS v4u*)(cp + 0 * CPY + 16 * a) = v4u{O1, O2, O3, O4};
      }
    }
  }
  STAMP(5);
  {
    asm volatile("s_waitcnt vmcnt(0)" : "+v"(ua), "+v"(ub_)::"memory");
    LAS unsigned char* q = L + B_U + (tid & 1) * U_CH + 512 + (tid >> 1) * 16;
    *(LAS v4u*)q = ua;
    *(LAS v4u*)(q + 2048) = ub_;
  }
  STAMP(6);
  BAR();
  if (tid == 0) set_flag(p.ws, WS_F2 + (g * 32 + m) * 128, tag);
  STAMP(7);
  f32x4 acc[8];
#pragma unroll
  for (int i = 0; i < 8; ++i) acc[i] = f32x4{0, 0, 0, 0};
  {
    LAS unsigned char* ab = L + ch * CPY_CH + (lr & 3) * CPY + 2 * (2032 - 1792 - 192 + 8 * lq - 4 * (lr >> 2) - 32 * (w & 1));
    LAS unsigned char* ub = L + B_U + ch * U_CH + 512 + 2 * (16 * lr - 16 + 8 * lq - 192 - 32 * (w & 1));
    v4u afA[8], bfA[8], afB[8], bfB[8];
    auto ldc = [&](v4u (&af)[8], v4u (&bf)[8], int rr) {
#pragma unroll
      for (int i = 0; i < 8; ++i) {
        bf[i] = *(LAS v4u*)(ub + 128 * (3 - rr) + 512 * i);
        v2u x0, x1;
        asm volatile("" ::: "memory");
        x0 = *(LAS v2u*)(ab + 128 * (3 - rr) + 512 * (7 - i));
        asm volatile("" ::: "memory");
        x1 = *(LAS v2u*)(ab + 128 * (3 - rr) + 512 * (7 - i) + 8);
        af[i] = v4u{x0.x, x0.y, x1.x, x1.y};
      }
    };
    auto mmc = [&](v4u (&af)[8], v4u (&bf)[8]) {
#pragma unroll
      for (int sm = 0; sm < 8; ++sm)
#pragma unroll
        for (int o = 0; o <= sm; ++o) {
          if (sm + o < 8) acc[sm + o] = mfma_bf16(af[sm], bf[o], acc[sm + o]);
          if (o < sm && sm + o < 8) acc[sm + o] = mfma_bf16(af[o], bf[sm], acc[sm + o]);
        }
    };
    ldc(afA, bfA, 0);
    SB0();
#pragma unroll
    for (int rr = 0; rr < 4; rr += 2) {
      ldc(afB, bfB, rr + 1);
      mmc(afA, bfA);
#pragma unroll
      for (int i = 0; i < 24; ++i) { SGB(0x8, 1); SGB(0x100, 1); }
      SB0();
      if (rr + 2 < 4) ldc(afA, bfA, rr + 2);
      mmc(afB, bfB);
#pragma unroll
      for (int i = 0; i < 24; ++i) { SGB(0x8, 1); SGB(0x100, 1); }
      SB0();
    }
  }
  STAMP(8);
  v2u fl2;
  const unsigned f2vo = (unsigned)((4 * w + (lane & 3) + 16 * ((lane >> 2) & 1)) * 128);
  bld8(fl2, rs, f2vo, WS_F2 + g * 4096);
  {
    LAS unsigned char* X = L + 36864 + w * 4096;
    const int keep = (w & 1) * 4;
    f32x4 mine[4];
#pragma unroll
    for (int i = 0; i < 4; ++i) {
      f32x4 lo = acc[i], hi = acc[4 + i];
      asm("" : "+v"(lo), "+v"(hi));
      *(LAS f32x4*)(X + i * 1024 + lane * 16) = (w & 1) ? lo : hi;
      mine[i] = (w & 1) ? hi : lo;
    }
    BAR();
    LAS unsigned char* Y = L + 36864 + (w ^ 1) * 4096;
    v2u yo[4];
#pragma unroll
    for (int i = 0; i < 4; ++i) {
      const f32x4 s = mine[i] + *(LAS f32x4*)(Y + i * 1024 + lane * 16);
      yo[i].x = nosent(pk_bf16(s[0], s[1]));
      yo[i].y = nosent(pk_bf16(s[2], s[3]));
    }
    const unsigned dst = WS_YT + (d * 8 + keep) * 512;
    const unsigned vo = (unsigned)((lq & 1) * 512 + (lq >> 1) * 256 + lr * 16);
#pragma unroll
    for (int i = 0; i < 4; i += 2) {
      auto r0 = __builtin_amdgcn_permlane16_swap(yo[i].x, yo[i + 1].x, false, false);
      auto r1 = __builtin_amdgcn_permlane16_swap(yo[i].y, yo[i + 1].y, false, false);
      bst16<0>(v4u{r0[0], r1[0], r0[1], r1[1]}, rs, vo, dst + i * 512);
    }
  }
  STAMP(9);
  {
    const int c0 = tid >> 3, hh = (tid >> 2) & 1, l4 = tid & 3;
    const unsigned src = WS_YT + (64 * g * 8 + (m >> 2)) * 512 + (m & 3) * 64;
    asm volatile("s_waitcnt vmcnt(0)" : "+v"(fl2)::"memory");
    {
      unsigned sp = 0;
      while (!wall((((u64)fl2.y << 32) | fl2.x) == tag) && ++sp < SPIN_CAP) {
        __builtin_amdgcn_s_sleep(8);
        bld8(fl2, rs, f2vo, WS_F2 + g * 4096);
        asm volatile("s_waitcnt vmcnt(0)" : "+v"(fl2)::"memory");
      }
    }
    const unsigned vo = (unsigned)(c0 * 4096 + hh * 256 + l4 * 16);
    v4u a0, a1;
    unsigned spins = 0;
    for (;;) {
      bld16<0>(a0, rs, vo, src);
      bld16<0>(a1, rs, vo, src + 32 * 4096);
      asm volatile("s_waitcnt vmcnt(0)" : "+v"(a0), "+v"(a1)::"memory");
      if (!wany(has_sent(a0) || has_sent(a1)) || ++spins >= SPIN_CAP) break;
      __builtin_amdgcn_s_sleep(6);
    }
    STAMP(10);
    if (m == 0 && tid == 0) set_flag(p.ws, WS_EPB + 256 * g, tag);
    LAS unsigned char* q = L + c0 * 128 + l4 * 32 + hh * 16;
    *(LAS v4u*)q = a0;
    *(LAS v4u*)(q + 4096) = a1;
  }
  BAR();
  {
    LAS unsigned char* q = L + (8 * lq + ((lane >> 2) & 3)) * 128 + 32 * w + 8 * (lane & 3);
    GAS unsigned char* op = (GAS unsigned char*)(p.out + (size_t)(64 * m + 16 * w + lr) * 1024 + 512 + 64 * g + 8 * lq);
#pragma unroll
    for (int it = 0; it < 2; ++it) *(GAS v4u*)(op + it * 64) = tr_read2(q + it * 4096, q + it * 4096 + 512);
  }
  STAMP(11);
  STAMP_END(12);
}

__global__ void __launch_bounds__(256, 2) fused_kernel(Params p) {
  extern __shared__ __attribute__((aligned(16))) unsigned char smem[];
  LAS unsigned char* L = (LAS unsigned char*)smem;
  asm volatile("" ::"s"(p.x), "s"(p.wqkv), "s"(p.wo), "s"(p.u), "s"(p.lar), "s"(p.aim), "s"(p.cre), "s"(p.cim), "s"(p.ldt), "s"(p.dsk), "s"(p.out), "s"(p.ws));
  const unsigned char* code = (const unsigned char*)(__builtin_amdgcn_s_getpc() & ~(u64)255);
  STAMP_DECL
  STAMP(0);
  int bx = blockIdx.x;
  const int idx = blockIdx.x & 255;
  if (bx >= 256) roleA_first(p, L, idx);
  asm volatile("" : "+s"(bx));
  if (bx >= 256) roleA(p, L, idx STAMP_PASS);
  else { BIn in; roleB_first(p, idx, code, in); roleB(p, L, idx, in STAMP_PASS); }
}

#if defined(__HIP_DEVICE_COMPILE__)
#pragma clang attribute pop
#endif

extern "C" void kernel_launch(void* const* d_in, const int* in_sizes, int n_in, void* d_out, int out_size, void* d_ws,
                              size_t ws_size, hipStream_t stream) {
  Params p;
  p.x = (const u16*)d_in[0];
  p.wqkv = (const u16*)d_in[1];
  p.wo = (const u16*)d_in[2];
  p.u = (const u16*)d_in[3];
  p.lar = (const u16*)d_in[4];
  p.aim = (const u16*)d_in[5];
  p.cre = (const u16*)d_in[6];
  p.cim = (const u16*)d_in[7];
  p.ldt = (const u16*)d_in[8];
  p.dsk = (const u16*)d_in[9];
  p.out = (u16*)d_out;
  p.ws = (unsigned char*)d_ws;
  (void)hipFuncSetAttribute((const void*)fused_kernel, hipFuncAttributeMaxDynamicSharedMemorySize, LDS_BYTES);
  fused_kernel<<<512, 256, LDS_BYTES, stream>>>(p);
}
```

```cpp
#include <hip/hip_runtime.h>
#include <stdint.h>

#if defined(__HIP_DEVICE_COMPILE__)
#pragma clang attribute push(__attribute__((target("no-packed-fp32-ops"))), apply_to = function)
#endif
#define LAS __attribute__((address_space(3)))
#define GAS __attribute__((address_space(1)))
#define DEV __device__ __forceinline__
typedef unsigned short u16;
typedef unsigned long long u64;
typedef unsigned v4u __attribute__((ext_vector_type(4)));
typedef unsigned v2u __attribute__((ext_vector_type(2)));
typedef short s16x4 __attribute__((ext_vector_type(4)));
typedef __bf16 bf16x8 __attribute__((ext_vector_type(8)));
typedef __bf16 bf16x2 __attribute__((ext_vector_type(2)));
typedef _Float16 f16x2 __attribute__((ext_vector_type(2)));
typedef _Float16 f16x8 __attribute__((ext_vector_type(8)));
typedef float f32x4 __attribute__((ext_vector_type(4)));
typedef float f32x2 __attribute__((ext_vector_type(2)));
typedef GAS u64 gu64;

#define STAMPS 0

#define GATHER_AT 4
constexpr int WS_EPA = 0;
constexpr int WS_EPB = 256;
constexpr int WS_FA = 4096;
constexpr int WS_F1 = WS_FA + 256 * 128;
constexpr int WS_F2 = WS_F1 + 256 * 128;
constexpr int KVP_REC = 3072;
constexpr int WS_KVP = WS_F2 + 256 * 128;
constexpr int WS_O = WS_KVP + 256 * KVP_REC;
constexpr int WS_UT = WS_O + 2 * 1024 * 1024;
constexpr int WS_YT = WS_UT + 2 * 1024 * 1024;
constexpr int WS_STAMP = WS_YT + 2 * 1024 * 1024;
constexpr int WS_BYTES = WS_STAMP + 512 * 4 * 16 * 8;
constexpr int LDS_BYTES = 80 * 1024;
constexpr int CODE_BYTES = 24320;
constexpr unsigned SPIN_CAP = 1u << 16;

struct Params {
  const u16 *x, *wqkv, *wo, *u, *lar, *aim, *cre, *cim, *ldt, *dsk;
  u16* out;
  unsigned char* ws;
};

DEV float u2f(unsigned v) { return __builtin_bit_cast(float, v); }
DEV bool wany(bool c) { return __builtin_amdgcn_ballot_w64(c) != 0; }
DEV bool wall(bool c) { return __builtin_amdgcn_ballot_w64(!c) == 0; }
DEV float bf2f(unsigned v) { return u2f(v << 16); }
DEV unsigned pk_bf16(float a, float b) {
  f32x2 v = {a, b};
  return __builtin_bit_cast(unsigned, __builtin_convertvector(v, bf16x2));
}
DEV unsigned pk_f16(float a, float b) {
  f32x2 v = {a, b};
  return __builtin_bit_cast(unsigned, __builtin_convertvector(v, f16x2));
}
DEV float rbf(float a) { return u2f(pk_bf16(a, a) << 16); }
DEV f32x4 mfma_bf16(v4u a, v4u b, f32x4 c) {
  return __builtin_amdgcn_mfma_f32_16x16x32_bf16(__builtin_bit_cast(bf16x8, a), __builtin_bit_cast(bf16x8, b), c, 0, 0, 0);
}
DEV f32x4 mfma_f16(v4u a, v4u b, f32x4 c) {
  return __builtin_amdgcn_mfma_f32_16x16x32_f16(__builtin_bit_cast(f16x8, a), __builtin_bit_cast(f16x8, b), c, 0, 0, 0);
}
DEV f32x4 mfma16_bf16(v2u a, v2u b, f32x4 c) {
  return __builtin_amdgcn_mfma_f32_16x16x16bf16_1k(__builtin_bit_cast(s16x4, a), __builtin_bit_cast(s16x4, b), c, 0, 0, 0);
}
DEV v2u tr_read(LAS unsigned char* p) {
  return __builtin_bit_cast(v2u, __builtin_amdgcn_ds_read_tr16_b64_v4i16((LAS s16x4*)p));
}
DEV v4u tr_read2(LAS unsigned char* p0, LAS unsigned char* p1) {
  v2u a = tr_read(p0), b = tr_read(p1);
  return v4u{a.x, a.y, b.x, b.y};
}
#define DRAIN() asm volatile("s_waitcnt vmcnt(0)" ::: "memory")
#define BAR() do { asm volatile("s_waitcnt lgkmcnt(0)" ::: "memory"); __builtin_amdgcn_s_barrier(); asm volatile("" ::: "memory"); } while (0)
#define RLX_AGENT __ATOMIC_RELAXED, __HIP_MEMORY_SCOPE_AGENT
constexpr int AUX_SC1 = 16;
typedef __amdgpu_buffer_rsrc_t rsrc_t;
DEV void set_flag(unsigned char* ws, int off, u64 tag) { __hip_atomic_store((gu64*)(ws + off), tag, RLX_AGENT); }

#define STAMP_ARGS
#define STAMP_PASS
#define STAMP_DECL
#define STAMP(k) do { } while (0)
#define STAMP_END(n) do { } while (0)

constexpr float PHI_EPS = 1e-6f;
template <int N> DEV void phi_n(float (&u)[N]) {
  float e[N];
#pragma unroll
  for (int i = 0; i < N; ++i) e[i] = __builtin_fmaf(u[i], 1.44269504088896341f, -1.44269504088896341e-6f);
#pragma unroll
  for (int i = 0; i < N; ++i) e[i] = __builtin_amdgcn_fmed3f(__builtin_amdgcn_exp2f(e[i]), 0.f, 1.f);
#pragma unroll
  for (int i = 0; i < N; ++i) asm("v_max_f32 %0, %1, %0" : "+v"(u[i]) : "s"(PHI_EPS));
#pragma unroll
  for (int i = 0; i < N; ++i) u[i] += e[i];
}
template <int V> struct IC { static constexpr int value = V; };
struct cf { float x, y; };
DEV cf cmul(cf a, cf b) { return cf{__builtin_fmaf(-a.y, b.y, a.x * b.x), __builtin_fmaf(a.y, b.x, a.x * b.y)}; }
DEV cf csq(cf a) { return cf{__builtin_fmaf(-a.y, a.y, a.x * a.x), (a.x + a.x) * a.y}; }
DEV unsigned nosent(unsigned v) { return min(v, 0xFFFFFFFEu); }
DEV v4u nosent4(v4u v) { return v4u{nosent(v.x), nosent(v.y), nosent(v.z), nosent(v.w)}; }
DEV unsigned umax3(unsigned a, unsigned b, unsigned c) { return max(max(a, b), c); }
DEV bool has_sent(v4u v) { return umax3(umax3(v.x, v.y, v.z), v.w, 0u) == 0xFFFFFFFFu; }
#define NTF ""
DEV rsrc_t mk_rsrc(const void* p, int bytes) { return __builtin_amdgcn_make_buffer_rsrc((void*)p, 0, bytes, 0x00020000); }
DEV void dma1(unsigned m0v, unsigned voff, rsrc_t rs, unsigned soff) {
  asm volatile("s_mov_b32 m0, %0\n\ts_nop 0\n\tbuffer_load_dwordx4 %1, %2, %3 offen lds" ::"s"(m0v), "v"(voff), "s"(rs), "s"(soff) : "memory");
}
DEV void dma2(unsigned m0v, unsigned voff, rsrc_t rs, unsigned soff) {
  asm volatile("s_mov_b32 m0, %0\n\ts_nop 0\n\tbuffer_load_dwordx4 %1, %2, %3 offen lds\n\tbuffer_load_dwordx4 %1, %2, %3 offen offset:1024 lds"
               ::"s"(m0v), "v"(voff), "s"(rs), "s"(soff) : "memory");
}
DEV void dma4nt(unsigned m0v, unsigned voff, rsrc_t rs, unsigned soff) {
  asm volatile("s_mov_b32 m0, %0\n\ts_nop 0\n\tbuffer_load_dwordx4 %1, %2, %3 offen" NTF " lds\n\tbuffer_load_dwordx4 %1, %2, %3 offen offset:1024" NTF " lds\n\t"
               "buffer_load_dwordx4 %1, %2, %3 offen offset:2048" NTF " lds\n\tbuffer_load_dwordx4 %1, %2, %3 offen offset:3072" NTF " lds"
               ::"s"(m0v), "v"(voff), "s"(rs), "s"(soff) : "memory");
}
template <int IMM> DEV void dma1nt(unsigned m0v, unsigned voff, rsrc_t rs, unsigned soff) {
  asm volatile("s_mov_b32 m0, %0\n\ts_nop 0\n\tbuffer_load_dwordx4 %1, %2, %3 offen offset:%4" NTF " lds" ::"s"(m0v), "v"(voff), "s"(rs), "s"(soff), "n"(IMM) : "memory");
}
template <int IMM> DEV void dma1i(unsigned m0v, unsigned voff, rsrc_t rs, unsigned soff) {
  asm volatile("s_mov_b32 m0, %0\n\ts_nop 0\n\tbuffer_load_dwordx4 %1, %2, %3 offen offset:%4 lds" ::"s"(m0v), "v"(voff), "s"(rs), "s"(soff), "n"(IMM) : "memory");
}
DEV void dma4v(unsigned m0v, const unsigned (&voff)[4], rsrc_t rs, unsigned soff) {
  asm volatile("s_mov_b32 m0, %0\n\ts_nop 0\n\tbuffer_load_dwordx4 %1, %5, %6 offen lds\n\tbuffer_load_dwordx4 %2, %5, %6 offen offset:1024 lds\n\t"
               "buffer_load_dwordx4 %3, %5, %6 offen offset:2048 lds\n\tbuffer_load_dwordx4 %4, %5, %6 offen offset:3072 lds"
               ::"s"(m0v), "v"(voff[0]), "v"(voff[1]), "v"(voff[2]), "v"(voff[3]), "s"(rs), "s"(soff) : "memory");
}
template <int IMM> DEV void bst16(v4u v, rsrc_t rs, unsigned voff, unsigned soff) {
  asm volatile("buffer_store_dwordx4 %0, %1, %2, %3 offen offset:%4 sc1\n\ts_nop 1" ::"v"(v), "v"(voff), "s"(rs), "s"(soff), "n"(IMM) : "memory");
}
template <int IMM> DEV void bld16(v4u& d, rsrc_t rs, unsigned voff, unsigned soff) {
  asm volatile("buffer_load_dwordx4 %0, %1, %2, %3 offen offset:%4 sc1" : "=v"(d) : "v"(voff), "s"(rs), "s"(soff), "n"(IMM) : "memory");
}
template <int IMM> DEV void bld16_re(v4u& d, rsrc_t rs, unsigned voff, unsigned soff) {
  asm volatile("buffer_load_dwordx4 %0, %1, %2, %3 offen offset:%4 sc1" ::"v"(d), "v"(voff), "s"(rs), "s"(soff), "n"(IMM) : "memory");
}
DEV void bld8(v2u& d, rsrc_t rs, unsigned voff, unsigned soff) {
  asm volatile("buffer_load_dwordx2 %0, %1, %2, %3 offen sc1" : "=v"(d) : "v"(voff), "s"(rs), "s"(soff) : "memory");
}
template <int N> DEV void vmwait() { asm volatile("s_waitcnt vmcnt(%0)" ::"n"(N) : "memory"); }
#define SB0() __builtin_amdgcn_sched_barrier(0)
#define SGB(mask, n) __builtin_amdgcn_sched_group_barrier(mask, n, 0)

#define A_SETUP() \
  const int tid = threadIdx.x, lane = tid & 63, w = __builtin_amdgcn_readfirstlane(tid >> 6); \
  const int g = a & 7, m = a >> 3; \
  const int bi = 4 * (g >> 1) + (m >> 3), bj = 8 * (g & 1) + (m & 7); \
  const unsigned Lb = (unsigned)(uintptr_t)L; \
  const rsrc_t rx = mk_rsrc(p.x, 2048 * 512 * 2), rw = mk_rsrc(p.wqkv, 512 * 1536 * 2); \
  const int rot = ((m & 7) + 2 * (m >> 3)) & 7; \
  const unsigned wvoff = (unsigned)(((16 * w + (lane >> 2)) * 1536 + 32 * bj + 8 * ((lane & 3) ^ ((lane >> 5) << 1))) * 2); \
  const unsigned xvoff = (unsigned)((128 * bi + 32 * w + 4 * (lane >> 3)) * 1024 + (((lane & 7) ^ (lane >> 3)) * 16));
DEV void roleA_first(const Params& p, LAS unsigned char* L, const int a) {
  A_SETUP()
#pragma unroll
  for (int c = 0; c < 2; ++c) {
    const unsigned kc = (unsigned)((c + rot) & 7);
    dma2(Lb + 49152 + c * 8192 + w * 2048, wvoff, rw, kc * (64 * 3072) + 1024);
    dma4nt(Lb + c * 16384 + w * 4096, xvoff, rx, kc * 128);
  }
}
#define APRIO 2
DEV void roleA(const Params& p, LAS unsigned char* L, const int a STAMP_ARGS) {
  A_SETUP()
  const int lr = lane & 15, lq = lane >> 4;
  const int R = 2 * bi + (bj >> 3), C = bj & 7;
  __builtin_amdgcn_s_setprio(APRIO);
  const rsrc_t ro = mk_rsrc(p.wo, 512 * 512 * 2), rs = mk_rsrc(p.ws, WS_BYTES);
  STAMP(1);
  const unsigned l16 = lane * 16;
  const unsigned o_dst = WS_O + ((8 * bi + 2 * w) * 16 + bj) * 1024;
  const unsigned kv_dst = WS_KVP + (bj * 16 + bi) * KVP_REC + min(w, 2) * 1024;
  const int tbo = (8 * (lq & 1) + ((lane >> 2) & 3)) * 64 + 8 * (lane & 3);
  LAS unsigned char* tb0 = L + (lq >> 1) * 2048 + tbo + 32 * (lq & 1);
  LAS unsigned char* tb1 = L + (lq >> 1) * 2048 + tbo + 32 * ((lq & 1) ^ 1);
  LAS unsigned char* xr0 = L + w * 4096 + lr * 128 + ((lq ^ (lr & 7)) * 16);
  LAS unsigned char* xr1 = L + w * 4096 + lr * 128 + (((4 + lq) ^ (lr & 7)) * 16);
  const unsigned fa_voff = (unsigned)((bi * 16 + 4 * w + (lane & 3)) * 128);
  v2u ep, fl;
  v4u xf[8][4];
  f32x4 ak[2][2], av[2][2];
#pragma unroll
  for (int r = 0; r < 2; ++r)
#pragma unroll
    for (int h = 0; h < 2; ++h) { ak[r][h] = f32x4{PHI_EPS, PHI_EPS, PHI_EPS, PHI_EPS}; av[r][h] = f32x4{0, 0, 0, 0}; }
  auto kv_chunk = [&](auto cc) {
    constexpr int c = decltype(cc)::value;
    constexpr int wo_ = 49152 + (c % 3) * 8192, xo_ = (c % 3) * 16384;
    vmwait<(c < 7) ? 6 : 5>();
    __builtin_amdgcn_s_barrier();
    asm volatile("" ::: "memory");
    v4u wa[4], wb[4];
    xf[c][0] = *(LAS v4u*)(xr0 + xo_);
    wa[0] = tr_read2(tb0 + wo_, tb0 + wo_ + 256);
    wa[1] = tr_read2(tb1 + wo_, tb1 + wo_ + 256);
    xf[c][1] = *(LAS v4u*)(xr0 + xo_ + 2048);
    wa[2] = tr_read2(tb0 + wo_ + 1024, tb0 + wo_ + 1024 + 256);
    wa[3] = tr_read2(tb1 + wo_ + 1024, tb1 + wo_ + 1024 + 256);
    SB0();
    if constexpr (c == 6) {
      bld8(ep, rs, 0u, WS_EPA);
    }
    const unsigned kc = (unsigned)((c + 2 + rot) & 7);
    auto piece = [&](auto ee) {
      constexpr int e = decltype(ee)::value;
      if constexpr (c + 2 < 8) {
        if constexpr (e < 2) dma1i<e * 1024>(Lb + 49152 + ((c + 2) % 3) * 8192 + w * 2048, wvoff, rw, kc * (64 * 3072) + 1024);
        else dma1nt<(e - 2) * 1024>(Lb + ((c + 2) % 3) * 16384 + w * 4096, xvoff, rx, kc * 128);
      } else if constexpr (e < 4) {
        dma1(Lb + ((c - 4) % 3) * 16384 + e * 4096 + w * 1024, wvoff, rw, (unsigned)((4 * (c - 6) + e + rot) & 7) * (64 * 3072));
      }
      SB0();
    };
    piece(IC<0>{});
    xf[c][2] = *(LAS v4u*)(xr1 + xo_);
    wb[0] = tr_read2(tb0 + wo_ + 4096, tb0 + wo_ + 4096 + 256);
    wb[1] = tr_read2(tb1 + wo_ + 4096, tb1 + wo_ + 4096 + 256);
    ak[0][0] = mfma_bf16(xf[c][0], wa[0], ak[0][0]);
    ak[0][1] = mfma_bf16(xf[c][0], wa[1], ak[0][1]);
    SB0();
    piece(IC<1>{});
    xf[c][3] = *(LAS v4u*)(xr1 + xo_ + 2048);
    wb[2] = tr_read2(tb0 + wo_ + 5120, tb0 + wo_ + 5120 + 256);
    wb[3] = tr_read2(tb1 + wo_ + 5120, tb1 + wo_ + 5120 + 256);
    ak[1][0] = mfma_bf16(xf[c][1], wa[0], ak[1][0]);
    ak[1][1] = mfma_bf16(xf[c][1], wa[1], ak[1][1]);
    av[0][0] = mfma_bf16(xf[c][0], wa[2], av[0][0]);
    SB0();
    piece(IC<2>{});
    av[0][1] = mfma_bf16(xf[c][0], wa[3], av[0][1]);
    av[1][0] = mfma_bf16(xf[c][1], wa[2], av[1][0]);
    av[1][1] = mfma_bf16(xf[c][1], wa[3], av[1][1]);
    SB0();
    piece(IC<3>{});
    ak[0][0] = mfma_bf16(xf[c][2], wb[0], ak[0][0]);
    ak[0][1] = mfma_bf16(xf[c][2], wb[1], ak[0][1]);
    ak[1][0] = mfma_bf16(xf[c][3], wb[0], ak[1][0]);
    SB0();
    piece(IC<4>{});
    ak[1][1] = mfma_bf16(xf[c][3], wb[1], ak[1][1]);
    av[0][0] = mfma_bf16(xf[c][2], wb[2], av[0][0]);
    av[0][1] = mfma_bf16(xf[c][2], wb[3], av[0][1]);
    SB0();
    piece(IC<5>{});
    av[1][0] = mfma_bf16(xf[c][3], wb[2], av[1][0]);
    av[1][1] = mfma_bf16(xf[c][3], wb[3], av[1][1]);
    SB0();
  };
  kv_chunk(IC<0>{}); kv_chunk(IC<1>{}); kv_chunk(IC<2>{}); kv_chunk(IC<3>{});
  kv_chunk(IC<4>{}); kv_chunk(IC<5>{}); kv_chunk(IC<6>{}); kv_chunk(IC<7>{});
  STAMP(2);
  {
    const v4u sent = {0xFFFFFFFFu, 0xFFFFFFFFu, 0xFFFFFFFFu, 0xFFFFFFFFu};
    bst16<0>(sent, rs, l16, o_dst);
    bst16<0>(sent, rs, l16, o_dst + 16384);
  }
  LAS unsigned char* S = L + 73728;
  LAS unsigned char* SZ = L + 49152;
  {
    const v4u ones = {0x3F803F80u, 0x3F803F80u, 0x3F803F80u, 0x3F803F80u};
    float t[16];
#pragma unroll
    for (int i = 0; i < 16; ++i) t[i] = ak[(i >> 2) & 1][i >> 3][i & 3];
    phi_n<16>(t);
    f32x4 kv[2], zz[2];
#pragma unroll
    for (int h = 0; h < 2; ++h) {
      const v4u pk = {pk_bf16(t[8 * h], t[8 * h + 1]), pk_bf16(t[8 * h + 2], t[8 * h + 3]), pk_bf16(t[8 * h + 4], t[8 * h + 5]), pk_bf16(t[8 * h + 6], t[8 * h + 7])};
      const v4u vv = {pk_bf16(av[0][h][0], av[0][h][1]), pk_bf16(av[0][h][2], av[0][h][3]), pk_bf16(av[1][h][0], av[1][h][1]), pk_bf16(av[1][h][2], av[1][h][3])};
      kv[h] = mfma_bf16(pk, vv, f32x4{0, 0, 0, 0});
      zz[h] = mfma_bf16(ones, pk, f32x4{0, 0, 0, 0});
    }
    *(LAS f32x4*)(S + w * 2048 + l16) = kv[0];
    *(LAS f32x4*)(S + w * 2048 + 1024 + l16) = f32x4{kv[0][3], kv[1][0], kv[1][1], 0.f};
    *(LAS f32x4*)(SZ + w * 2048 + l16) = f32x4{kv[1][2], kv[1][3], (lq & 1) ? zz[1][0] : zz[0][0], 0.f};
  }
  LAS unsigned char* psrc = ((w < 2) ? S + w * 1024 : SZ) + l16;
  asm volatile("s_waitcnt vmcnt(10)" : "+v"(ep)::"memory");
  const u64 tag = (((u64)ep.y << 32) | ep.x) + 1;
  const unsigned tag32 = (unsigned)tag;
  BAR();
  {
    const f32x4 s0 = *(LAS f32x4*)(psrc), s1 = *(LAS f32x4*)(psrc + 2048), s2 = *(LAS f32x4*)(psrc + 4096), s3 = *(LAS f32x4*)(psrc + 6144);
    v4u o = __builtin_bit_cast(v4u, (s0 + s1) + (s2 + s3));
    o.w = tag32;
    bst16<0>(o, rs, l16, kv_dst);
  }
  STAMP(3);
  vmwait<3>();
  BAR();
  unsigned ovoff[4];
#pragma unroll
  for (int r = 0; r < 4; ++r) ovoff[r] = (unsigned)(4 * (lane >> 3) * 1024 + 128 * C + 16 * ((lane & 7) ^ (r << 1)));
  f32x4 aq[2][2];
#pragma unroll
  for (int r = 0; r < 2; ++r)
#pragma unroll
    for (int h = 0; h < 2; ++h) aq[r][h] = f32x4{PHI_EPS, PHI_EPS, PHI_EPS, PHI_EPS};
  v4u t0[4], t1[4], tz[4];
  auto gather = [&]() {
#pragma unroll
    for (int q = 0; q < 4; ++q) {
      const unsigned rec = WS_KVP + (bj * 16 + 4 * w + q) * KVP_REC;
      bld16<0>(t0[q], rs, l16, rec);
      bld16<1024>(t1[q], rs, l16, rec);
      bld16<2048>(tz[q], rs, l16, rec);
    }
  };
  auto gather_bad = [&]() {
    unsigned x = 0;
#pragma unroll
    for (int q = 0; q < 4; ++q) x |= (t0[q].w ^ tag32) | (t1[q].w ^ tag32) | (tz[q].w ^ tag32);
    return wany(x != 0);
  };
#define GATHER_WAIT() asm volatile("s_waitcnt vmcnt(0)" : "+v"(t0[0]), "+v"(t0[1]), "+v"(t0[2]), "+v"(t0[3]), "+v"(t1[0]), "+v"(t1[1]), "+v"(t1[2]), "+v"(t1[3]), "+v"(tz[0]), "+v"(tz[1]), "+v"(tz[2]), "+v"(tz[3])::"memory")
  {
    LAS unsigned char* qb0 = tb0 - (lq >> 1) * 1024;
    LAS unsigned char* qb1 = tb1 - (lq >> 1) * 1024;
    v4u qa[4], qb[4];
    auto ldq = [&](v4u (&q)[4], int c) {
      const int o = (c < 4) ? 32768 + c * 4096 : (c - 4) * 4096;
      q[0] = tr_read2(qb0 + o, qb0 + o + 256);
      q[1] = tr_read2(qb1 + o, qb1 + o + 256);
      q[2] = tr_read2(qb0 + o + 2048, qb0 + o + 2048 + 256);
      q[3] = tr_read2(qb1 + o + 2048, qb1 + o + 2048 + 256);
    };
    auto mmq = [&](v4u (&q)[4], int c) {
#pragma unroll
      for (int kk = 0; kk < 2; ++kk)
#pragma unroll
        for (int r = 0; r < 2; ++r) {
          aq[r][0] = mfma_bf16(q[2 * kk], xf[c][2 * kk + r], aq[r][0]);
          aq[r][1] = mfma_bf16(q[2 * kk + 1], xf[c][2 * kk + r], aq[r][1]);
        }
    };
    ldq(qa, 0);
    SB0();
#pragma unroll
    for (int c = 0; c < 8; c += 2) {
      if (c == 2) {
        vmwait<4>();
        __builtin_amdgcn_s_barrier();
        asm volatile("" ::: "memory");
        if (tid == 0) set_flag(p.ws, WS_FA + (bi * 16 + bj) * 128, tag);
        SB0();
      }
      if (c < 4) { dma4v(Lb + (c ? 57344 : 16384) + w * 4096, ovoff, ro, (unsigned)(2 * c + w) * 32768); SB0(); }
      ldq(qb, c + 1);
      mmq(qa, c);
#pragma unroll
      for (int i = 0; i < 8; ++i) { SGB(0x8, 1); SGB(0x100, 1); }
      SB0();
      if (c + 2 < 8) ldq(qa, c + 2);
      mmq(qb, c + 1);
#pragma unroll
      for (int i = 0; i < 8; ++i) { SGB(0x8, 1); SGB(0x100, 1); }
      SB0();
      if (c == GATHER_AT) { gather(); SB0(); }
    }
  }
  v2u pq[2][2];
  {
    float t[16];
#pragma unroll
    for (int i = 0; i < 16; ++i) t[i] = aq[i >> 3][(i >> 2) & 1][i & 3];
    phi_n<16>(t);
#pragma unroll
    for (int i = 0; i < 4; ++i) pq[i >> 1][i & 1] = v2u{pk_bf16(t[4 * i], t[4 * i + 1]), pk_bf16(t[4 * i + 2], t[4 * i + 3])};
  }
  asm volatile("" : "+v"(pq[0][0]), "+v"(pq[0][1]), "+v"(pq[1][0]), "+v"(pq[1][1]));
  STAMP(4);
  {
    unsigned spins = 0;
    GATHER_WAIT();
    while (gather_bad() && ++spins < SPIN_CAP) {
      __builtin_amdgcn_s_sleep(4);
      gather();
      GATHER_WAIT();
    }
  }
  STAMP(5);
  {
    auto sum4 = [](const v4u (&t)[4], int e) { return (u2f(t[0][e]) + u2f(t[1][e])) + (u2f(t[2][e]) + u2f(t[3][e])); };
    *(LAS f32x4*)(S + w * 2048 + l16) = f32x4{sum4(t0, 0), sum4(t0, 1), sum4(t0, 2), sum4(t1, 0)};
    *(LAS f32x4*)(S + w * 2048 + 1024 + l16) = f32x4{sum4(t1, 1), sum4(t1, 2), sum4(tz, 0), sum4(tz, 1)};
    *(LAS float*)(SZ + w * 256 + lane * 4) = sum4(tz, 2);
    bld8(fl, rs, fa_voff, WS_FA);
  }
  BAR();
  STAMP(6);
  {
    f32x4 rs_[2][4], rz_[2][4];
#pragma unroll
    for (int h = 0; h < 2; ++h)
#pragma unroll
      for (int ww = 0; ww < 4; ++ww) {
        rs_[h][ww] = *(LAS f32x4*)(S + ww * 2048 + h * 1024 + l16);
        rz_[h][ww] = *(LAS f32x4*)(SZ + ww * 256 + h * 64 + lq * 16);
      }
    SB0();
    f32x4 num[2][2], den[2][2];
#pragma unroll
    for (int h = 0; h < 2; ++h) {
      const f32x4 s = (rs_[h][0] + rs_[h][1]) + (rs_[h][2] + rs_[h][3]);
      const f32x4 z = (rz_[h][0] + rz_[h][1]) + (rz_[h][2] + rz_[h][3]);
      const v2u kvb = v2u{pk_bf16(s[0], s[1]), pk_bf16(s[2], s[3])};
      const v2u zb = v2u{pk_bf16(z[0], z[1]), pk_bf16(z[2], z[3])};
#pragma unroll
      for (int r = 0; r < 2; ++r) {
        den[r][h] = mfma16_bf16(zb, pq[r][h], f32x4{1e-6f, 1e-6f, 1e-6f, 1e-6f});
        num[r][h] = mfma16_bf16(kvb, pq[r][h], f32x4{0, 0, 0, 0});
      }
    }
#pragma unroll
    for (int r = 0; r < 2; ++r) {
      const float r0 = __builtin_amdgcn_rcpf(den[r][0][0]), r1 = __builtin_amdgcn_rcpf(den[r][1][0]);
      const v4u ov = {pk_bf16(num[r][0][0] * r0, num[r][0][1] * r0), pk_bf16(num[r][0][2] * r0, num[r][0][3] * r0),
                      pk_bf16(num[r][1][0] * r1, num[r][1][1] * r1), pk_bf16(num[r][1][2] * r1, num[r][1][3] * r1)};
      bst16<0>(nosent4(ov), rs, l16, o_dst + r * 16384);
    }
  }
  STAMP(7);
  dma4v(Lb + w * 4096, ovoff, ro, (unsigned)(8 + w) * 32768);
  dma4v(Lb + 32768 + w * 4096, ovoff, ro, (unsigned)(12 + w) * 32768);
  const int T = 4 * R + w;
  const unsigned o_src = WS_O + T * 16384;
  const int ra = (lane >> 2) & 3;
  LAS unsigned char* ob = L + ra * 1024 + lq * 128 + 8 * (lane & 3);
  auto ldw = [&](v4u (&f)[4], int j) {
    const int off = (j < 4) ? 16384 + j * 4096 : (j < 8) ? 57344 + (j - 4) * 4096 : (j < 12) ? (j - 8) * 4096 : 32768 + (j - 12) * 4096;
#pragma unroll
    for (int c = 0; c < 4; ++c) {
      LAS unsigned char* q = ob + 32 * (c ^ ra) + off;
      f[c] = tr_read2(q, q + 512);
    }
  };
  v4u wf[8][4];
#pragma unroll
  for (int j = 0; j < 8; ++j) ldw(wf[j], j);
  asm volatile("s_waitcnt vmcnt(0)" : "+v"(fl)::"memory");
  {
    unsigned spins = 0;
    while (!wall((((u64)fl.y << 32) | fl.x) == tag) && ++spins < SPIN_CAP) {
      __builtin_amdgcn_s_sleep(8);
      bld8(fl, rs, fa_voff, WS_FA);
      asm volatile("s_waitcnt vmcnt(0)" : "+v"(fl)::"memory");
    }
  }
  BAR();
  STAMP(8);
  v4u of[16];
  f32x4 acc[4];
  {
    auto ldo = [&](auto jj) { constexpr int j = decltype(jj)::value; bld16<(j & 3) * 1024>(of[j], rs, l16, o_src + (j >> 2) * 4096); };
    ldo(IC<0>{}); ldo(IC<1>{}); ldo(IC<2>{}); ldo(IC<3>{}); ldo(IC<4>{}); ldo(IC<5>{}); ldo(IC<6>{}); ldo(IC<7>{});
    ldo(IC<8>{}); ldo(IC<9>{}); ldo(IC<10>{}); ldo(IC<11>{}); ldo(IC<12>{}); ldo(IC<13>{}); ldo(IC<14>{}); ldo(IC<15>{});
    STAMP(9);
    unsigned spins = 0;
    for (;;) {
      unsigned bad = 0;
#pragma unroll
      for (int c = 0; c < 4; ++c) acc[c] = f32x4{0, 0, 0, 0};
      auto step = [&](auto jj, v4u (&f)[4]) {
        constexpr int j = decltype(jj)::value;
        asm volatile("s_waitcnt vmcnt(%1)" : "+v"(of[j]) : "n"(15 - j) : "memory");
#pragma unroll
        for (int c = 0; c < 4; ++c) acc[c] = mfma_bf16(f[c], of[j], acc[c]);
        bad |= wany(has_sent(of[j])) ? (1u << j) : 0u;
        SB0();
      };
      v4u fa[4], fb[4];
      step(IC<0>{}, wf[0]); step(IC<1>{}, wf[1]); step(IC<2>{}, wf[2]); step(IC<3>{}, wf[3]);
      step(IC<4>{}, wf[4]); step(IC<5>{}, wf[5]);
      ldw(fa, 8); step(IC<6>{}, wf[6]);
      ldw(fb, 9); step(IC<7>{}, wf[7]);
      step(IC<8>{}, fa); ldw(fa, 10); step(IC<9>{}, fb); ldw(fb, 11);
      step(IC<10>{}, fa); ldw(fa, 12); step(IC<11>{}, fb); ldw(fb, 13);
      step(IC<12>{}, fa); ldw(fa, 14); step(IC<13>{}, fb); ldw(fb, 15);
      step(IC<14>{}, fa); step(IC<15>{}, fb);
      if (bad == 0 || ++spins >= SPIN_CAP) break;
      auto re = [&](auto jj) { constexpr int j = decltype(jj)::value; if (bad & (1u << j)) bld16_re<(j & 3) * 1024>(of[j], rs, l16, o_src + (j >> 2) * 4096); };
      re(IC<0>{}); re(IC<1>{}); re(IC<2>{}); re(IC<3>{}); re(IC<4>{}); re(IC<5>{}); re(IC<6>{}); re(IC<7>{});
      re(IC<8>{}); re(IC<9>{}); re(IC<10>{}); re(IC<11>{}); re(IC<12>{}); re(IC<13>{}); re(IC<14>{}); re(IC<15>{});
      vmwait<0>();
    }
  }
  if (a == 0 && tid == 0) set_flag(p.ws, WS_EPA, tag);
  STAMP(10);
  {
    const int row = 32 * (T >> 1) + 4 * (lr & 7) + 2 * (T & 1) + (lr >> 3);
    GAS unsigned char* op = (GAS unsigned char*)(p.out + (size_t)row * 1024 + 64 * C) + (lq & 1) * 32 + (lq >> 1) * 16;
#pragma unroll
    for (int c = 0; c < 4; c += 2) {
      unsigned a0 = pk_bf16(acc[c][0], acc[c][1]), a1 = pk_bf16(acc[c][2], acc[c][3]);
      unsigned b0 = pk_bf16(acc[c + 1][0], acc[c + 1][1]), b1 = pk_bf16(acc[c + 1][2], acc[c + 1][3]);
      auto r0 = __builtin_amdgcn_permlane16_swap(a0, b0, false, false);
      auto r1 = __builtin_amdgcn_permlane16_swap(a1, b1, false, false);
      *(GAS v4u*)(op + c * 32) = v4u{r0[0], r1[0], r0[1], r1[1]};
    }
  }
  STAMP(11);
  STAMP_END(12);
}

#define FLAG_AT 3
constexpr int TQ = 57 * 16;
constexpr int TAB_CH = 32 * TQ;
constexpr int B_KX = 2 * TAB_CH;
constexpr int KX_CH = 4352;
constexpr int B_U = B_KX + 2 * KX_CH;
constexpr int U_CH = 4608;
constexpr int CPY = 4160;
constexpr int CPY_CH = 4 * CPY;
static_assert(B_U + 2 * U_CH <= LDS_BYTES, "lds");

DEV cf cpow_direct(float al, float be, float pw) {
  const float mag = __builtin_amdgcn_exp2f(al * pw);
  const float ph = __builtin_amdgcn_fractf(be * pw);
  cf r = cf{mag * __builtin_amdgcn_cosf(ph), mag * __builtin_amdgcn_sinf(ph)};
  return r;
}
template <int CTRL, int RM> DEV unsigned dpp_max(unsigned v) {
  return max(v, (unsigned)__builtin_amdgcn_update_dpp((int)v, (int)v, CTRL, RM, 0xf, false));
}

struct BIn { v4u ut0, ut1; unsigned lar, aim, cre, cim, cre2, cim2, ldt, dsk, warm; u64 ep; };
DEV void roleB_first(const Params& p, const int b, const unsigned char* code, BIn& in) {
  const int tid = threadIdx.x;
  const int g = b & 7, m = b >> 3;
  const int d = 64 * g + 2 * m + (tid >> 7);
  const GAS v4u* up = (const GAS v4u*)(p.u + (size_t)(64 * m + (tid >> 3)) * 512 + 64 * g + 8 * (tid & 7));
  in.ut0 = *(up);
  in.ut1 = *(up + 32 * 64);
  const int e = (64 * g + 2 * m) * 128 + tid;
  in.lar = *(p.lar + e);
  in.aim = *(p.aim + e);
  in.cre = *(p.cre + e);
  in.cim = *(p.cim + e);
  in.cre2 = *(p.cre + (e ^ 64));
  in.cim2 = *(p.cim + (e ^ 64));
  in.ldt = p.ldt[d];
  in.dsk = p.dsk[d];
  in.ep = __hip_atomic_load((gu64*)(p.ws + WS_EPB + 256 * g), RLX_AGENT);
  in.warm = *(const GAS unsigned*)(code + min((m * 8 + (tid & 7)) * 128, CODE_BYTES - 128));
}
DEV void roleB(const Params& p, LAS unsigned char* L, const int b, const BIn& in STAMP_ARGS) {
  const int tid = threadIdx.x, lane = tid & 63, w = __builtin_amdgcn_readfirstlane(tid >> 6);
  const int lr = lane & 15, lq = lane >> 4;
  const int g = b & 7, m = b >> 3;
  const int ch = w >> 1;
  const int d = 64 * g + 2 * m + ch;
  const rsrc_t rs = mk_rsrc(p.ws, WS_BYTES);
  const unsigned l16 = lane * 16;
  const int n = tid & 127;
  const v4u ut0 = in.ut0, ut1 = in.ut1;
  const unsigned r_lar = in.lar, r_aim = in.aim, r_cre = in.cre, r_cim = in.cim, r_cre2 = in.cre2, r_cim2 = in.cim2, r_ldt = in.ldt, r_dsk = in.dsk;
  const unsigned warm = in.warm;
  const u64 tag = in.ep + 1;
  *(LAS v4u*)(L + (tid >> 3) * 128 + (tid & 7) * 16) = ut0;
  *(LAS v4u*)(L + 4096 + (tid >> 3) * 128 + (tid & 7) * 16) = ut1;
  if (w < 2) *(LAS v4u*)(L + ((w == 0) ? B_U + (lane >> 5) * U_CH + (lane & 31) * 16 : B_KX + ((lane >> 1) & 1) * KX_CH + (lane & 1) * 16)) = v4u{0, 0, 0, 0};
  BAR();
  STAMP(1);
  {
    LAS unsigned char* q = L + (8 * lq + ((lane >> 2) & 3)) * 128 + 32 * w + 8 * (lane & 3);
    const v4u v0 = tr_read2(q, q + 512), v1 = tr_read2(q + 4096, q + 4096 + 512);
    const unsigned dst = WS_UT + ((16 * g + 4 * w) * 256 + 8 * m) * 64;
    const unsigned vo = (unsigned)((lr >> 2) * 16384 + lq * 64 + (lr & 3) * 16);
    asm volatile("" ::"v"(r_lar), "v"(r_aim), "v"(r_cre), "v"(r_cim), "v"(r_cre2), "v"(r_cim2), "v"(r_ldt), "v"(r_dsk), "v"(tag), "v"(warm));
    __builtin_amdgcn_s_setprio(3);
    bst16<0>(v0, rs, vo, dst);
    bst16<256>(v1, rs, vo, dst);
    __builtin_amdgcn_s_setprio(0);
  }
  STAMP(2);
  float kscale;
  {
    const float dt = rbf(expf(bf2f(r_ldt)));
    const float ar = -rbf(expf(bf2f(r_lar))) * dt;
    const float al = ar * 1.44269504088896341f;
    const float be = (bf2f(r_aim) * dt) * 0.15915494309189535f;
    unsigned mx = max(max(r_cre & 0x7FFFu, r_cim & 0x7FFFu), max(r_cre2 & 0x7FFFu, r_cim2 & 0x7FFFu));
    mx = dpp_max<0x111, 0xf>(mx); mx = dpp_max<0x112, 0xf>(mx); mx = dpp_max<0x114, 0xf>(mx); mx = dpp_max<0x118, 0xf>(mx);
    mx = dpp_max<0x142, 0xa>(mx); mx = dpp_max<0x143, 0xc>(mx);
    int ex = __builtin_amdgcn_readlane((int)mx, 63) >> 7;
    ex = min(max(ex, 1), 253);
    const float cs = u2f((unsigned)(254 - ex) << 23);
    kscale = 2.f * dt * u2f((unsigned)ex << 23);
    const cf z1 = cpow_direct(al, be, 1.f), z8 = cpow_direct(al, be, 8.f), z128 = cpow_direct(al, be, 128.f);
    unsigned tv[57];
#define PUT(row, v) do { const cf v_ = (v); tv[row] = pk_f16(v_.x, v_.y); } while (0)
    cf zp[8];
    zp[1] = z1; zp[2] = csq(z1); zp[3] = cmul(zp[2], z1); zp[4] = csq(zp[2]);
    zp[5] = cmul(zp[4], z1); zp[6] = csq(zp[3]); zp[7] = cmul(zp[6], z1);
    tv[0] = 0x00003C00u;
#pragma unroll
    for (int i = 1; i < 8; ++i) { PUT(i, zp[i]); PUT(8 + i, cmul(zp[i], z128)); }
    PUT(8, z128);
    asm volatile("" : "+v"(tv[1]), "+v"(tv[2]), "+v"(tv[3]), "+v"(tv[4]), "+v"(tv[5]), "+v"(tv[6]), "+v"(tv[7]), "+v"(tv[8]), "+v"(tv[9]), "+v"(tv[10]), "+v"(tv[11]), "+v"(tv[12]), "+v"(tv[13]), "+v"(tv[14]), "+v"(tv[15]));
    DRAIN();
    BAR();
    __builtin_amdgcn_s_setprio(3);
    if (tid == 0) set_flag(p.ws, WS_F1 + (g * 32 + m) * 128, tag);
    __builtin_amdgcn_s_setprio(0);
    LAS unsigned char* tp = L + ch * TAB_CH + (n >> 2) * TQ + (n & 3) * 4;
#define WR(row) *(LAS unsigned*)(tp + (row) * 16) = tv[row]
#pragma unroll
    for (int i = 0; i < 16; ++i) WR(i);
    cf H[17];
    H[1] = z128;
#pragma unroll
    for (int i = 2; i <= 16; ++i) H[i] = (i & 1) ? cmul(H[i - 1], z128) : csq(H[i / 2]);
#pragma unroll
    for (int i = 2; i < 4; ++i) {
      PUT(16 + 4 * (i - 2), H[i]);
#pragma unroll
      for (int q = 1; q < 4; ++q) PUT(16 + 4 * (i - 2) + q, cmul(H[i], zp[2 * q]));
    }
#pragma unroll
    for (int i = 4; i < 8; ++i) { PUT(24 + 2 * (i - 4), H[i]); PUT(25 + 2 * (i - 4), cmul(H[i], zp[4])); }
#pragma unroll
    for (int i = 8; i <= 16; ++i) PUT(24 + i, H[i]);
#pragma unroll
    for (int i = 16; i < 40; ++i) WR(i);
    const cf z8c = cf{z8.x, -z8.y}, z16c = csq(z8c);
    cf f0 = cf{bf2f(r_cre) * cs, -(bf2f(r_cim) * cs)};
    cf f1 = cmul(f0, z8c);
#pragma unroll
    for (int i = 0; i < 8; ++i) {
      PUT(41 + 2 * i, f0);
      PUT(42 + 2 * i, f1);
      if (i < 7) { f0 = cmul(f0, z16c); f1 = cmul(f1, z16c); }
    }
#pragma unroll
    for (int i = 40; i < 57; ++i) WR(i);
#undef PUT
#undef WR
  }
  BAR();
  STAMP(3);
  {
    LAS unsigned char* tb = L + ch * TAB_CH + lq * TQ;
    LAS unsigned char* kx = L + B_KX + ch * KX_CH + 32;
    LAS unsigned char* fb = tb + (41 + lr) * 16;
    if ((w & 1) == 0) {
      {
        const v4u sent = {0xFFFFFFFFu, 0xFFFFFFFFu, 0xFFFFFFFFu, 0xFFFFFFFFu};
        bst16<0>(sent, rs, l16, WS_YT + d * 4096);
        bst16<1024>(sent, rs, l16, WS_YT + d * 4096);
        bst16<2048>(sent, rs, l16, WS_YT + d * 4096);
        bst16<3072>(sent, rs, l16, WS_YT + d * 4096);
      }
      LAS unsigned char* g0 = tb + lr * 16;
      LAS unsigned char* g2 = tb + (32 + min(lr, 8)) * 16;
      f32x4 c0 = {0, 0, 0, 0}, c2 = {0, 0, 0, 0};
      v4u fv[8], a0[8], a2[8];
#pragma unroll
      for (int s = 0; s < 8; ++s) {
        fv[s] = *(LAS v4u*)(fb + s * 4 * TQ);
        a0[s] = *(LAS v4u*)(g0 + s * 4 * TQ);
        a2[s] = *(LAS v4u*)(g2 + s * 4 * TQ);
      }
      SB0();
#pragma unroll
      for (int s = 0; s < 8; ++s) {
        c0 = mfma_f16(a0[s], fv[s], c0);
        c2 = mfma_f16(a2[s], fv[s], c2);
      }
      c0 *= kscale;
      c2 *= kscale;
      if (lane == 0) c0[0] += bf2f(r_dsk);
      *(LAS v2u*)(kx + 2 * (128 * (lq >> 1) + 8 * lr + 4 * (lq & 1))) = v2u{pk_bf16(c0[0], c0[1]), pk_bf16(c0[2], c0[3])};
#pragma unroll
      for (int r = 0; r < 4; ++r) {
        const unsigned vv = pk_bf16(c2[r], c2[r]);
        const unsigned pv = (unsigned)__builtin_amdgcn_update_dpp(0, (int)vv, 0x111, 0xf, 0xf, false);
        const unsigned first = (lr & 1) ? ((pv & 0xFFFFu) | (vv & 0xFFFF0000u)) : vv;
        const int pp = 128 * (8 + 4 * lq + r) + 8 * lr;
        if (lq < 2) {
          if (pp != 1024) *(LAS unsigned*)(kx + 2 * (pp - 4)) = first;
          *(LAS unsigned*)(kx + 2 * (pp - 2)) = vv;
          *(LAS v2u*)(kx + 2 * pp) = v2u{vv, vv};
        } else if (pp == 2048) {
          *(LAS v2u*)(kx + 2 * (pp - 4)) = v2u{vv, vv};
        }
      }
    } else {
      v2u fl;
      const unsigned fvo = (unsigned)((w >> 1) * 16 + lr) * 128;
      __builtin_amdgcn_s_setprio(3);
      bld8(fl, rs, fvo, WS_F1 + g * 4096);
      __builtin_amdgcn_s_setprio(0);
      LAS unsigned char* g1 = tb + (16 + lr) * 16;
      f32x4 c1 = {0, 0, 0, 0};
      v4u fv[8], a1[8];
#pragma unroll
      for (int s = 0; s < 8; ++s) {
        fv[s] = *(LAS v4u*)(fb + s * 4 * TQ);
        a1[s] = *(LAS v4u*)(g1 + s * 4 * TQ);
      }
      SB0();
#pragma unroll
      for (int s = 0; s < 8; ++s) c1 = mfma_f16(a1[s], fv[s], c1);
      c1 *= kscale;
      if (lq < 2) {
        const int base = 256 + 128 * lq + 8 * lr;
        const unsigned d0 = pk_bf16(c1[0], c1[0]), d1 = pk_bf16(c1[1], c1[2]), d2 = pk_bf16(c1[2], c1[2]), d3 = pk_bf16(c1[3], c1[3]);
        if (base != 256) *(LAS u16*)(kx + 2 * (base - 1)) = (u16)d0;
        *(LAS v2u*)(kx + 2 * base) = v2u{d0, d1};
        *(LAS unsigned*)(kx + 2 * (base + 4)) = d2;
        *(LAS u16*)(kx + 2 * (base + 6)) = (u16)d3;
      } else {
#pragma unroll
        for (int e = 0; e < 2; ++e) {
          const int base = 128 * (4 + 2 * (lq - 2) + e) + 8 * lr;
          const unsigned d0 = pk_bf16(c1[2 * e], c1[2 * e]), d1 = pk_bf16(c1[2 * e], c1[2 * e + 1]), d2 = pk_bf16(c1[2 * e + 1], c1[2 * e + 1]);
          if (base != 512) *(LAS unsigned*)(kx + 2 * (base - 2)) = d0;
          else *(LAS u16*)(kx + 2 * (base - 1)) = (u16)d0;
          *(LAS v2u*)(kx + 2 * base) = v2u{d0, d1};
          *(LAS unsigned*)(kx + 2 * (base + 4)) = d2;
        }
      }
      {
        unsigned spins = 0;
        asm volatile("s_waitcnt vmcnt(0)" : "+v"(fl)::"memory");
        while (!wall((((u64)fl.y << 32) | fl.x) == tag) && ++spins < SPIN_CAP) {
          __builtin_amdgcn_s_sleep(8);
          bld8(fl, rs, fvo, WS_F1 + g * 4096);
          asm volatile("s_waitcnt vmcnt(0)" : "+v"(fl)::"memory");
        }
      }
    }
  }
  BAR();
  v4u ua, ub_;
  {
    const unsigned src = WS_UT + (16 * g + (m >> 1)) * 16384 + (m & 1) * 32;
    const unsigned vo = (unsigned)((tid >> 1) * 64 + (tid & 1) * 16);
    __builtin_amdgcn_s_setprio(3);
    bld16<0>(ua, rs, vo, src);
    bld16<0>(ub_, rs, vo, src + 8192);
    __builtin_amdgcn_s_setprio(0);
  }
  STAMP(4);
  {
    LAS unsigned char* kx = L + B_KX + ch * KX_CH + 32;
    LAS unsigned char* cp = L + ch * CPY_CH;
    v4u lo[3];
    v2u hi[3];
#pragma unroll
    for (int it = 0; it < 3; ++it) {
      const int a = (it < 2) ? n + 128 * it : 256 + (n & 1);
      const int L0 = 2048 - 8 * a;
      lo[it] = *(LAS v4u*)(kx + 2 * (L0 - 8));
      hi[it] = *(LAS v2u*)(kx + 2 * L0);
    }
    SB0();
#pragma unroll
    for (int it = 0; it < 3; ++it) {
      const int a = (it < 2) ? n + 128 * it : 256 + (n & 1);
      if (it < 2 || n < 2) {
        const v4u l = lo[it];
        const v2u hh = hi[it];
        const unsigned E0 = __builtin_amdgcn_alignbit(hh.y, hh.y, 16), E1 = __builtin_amdgcn_alignbit(hh.x, hh.x, 16);
        const unsigned E2 = __builtin_amdgcn_alignbit(l.w, l.w, 16), E3 = __builtin_amdgcn_alignbit(l.z, l.z, 16);
        const unsigned E4 = __builtin_amdgcn_alignbit(l.y, l.y, 16);
        const unsigned O0 = (hh.y & 0xFFFFu) | (hh.x & 0xFFFF0000u), O1 = (hh.x & 0xFFFFu) | (l.w & 0xFFFF0000u);
        const unsigned O2 = (l.w & 0xFFFFu) | (l.z & 0xFFFF0000u), O3 = (l.z & 0xFFFFu) | (l.y & 0xFFFF0000u);
        const unsigned O4 = (l.y & 0xFFFFu) | (l.x & 0xFFFF0000u);
        *(LAS v4u*)(cp + 3 * CPY + 16 * a) = v4u{E0, E1, E2, E3};
        *(LAS v4u*)(cp + 2 * CPY + 16 * a) = v4u{O0, O1, O2, O3};
        *(LAS v4u*)(cp + 1 * CPY + 16 * a) = v4u{E1, E2, E3, E4};
        *(LAS v4u*)(cp + 0 * CPY + 16 * a) = v4u{O1, O2, O3, O4};
      }
    }
  }
  STAMP(5);
  {
    asm volatile("s_waitcnt vmcnt(0)" : "+v"(ua), "+v"(ub_)::"memory");
    LAS unsigned char* q = L + B_U + (tid & 1) * U_CH + 512 + (tid >> 1) * 16;
    *(LAS v4u*)q = ua;
    *(LAS v4u*)(q + 2048) = ub_;
  }
  STAMP(6);
  BAR();
  if (tid == 0) set_flag(p.ws, WS_F2 + (g * 32 + m) * 128, tag);
  STAMP(7);
  f32x4 acc[8];
#pragma unroll
  for (int i = 0; i < 8; ++i) acc[i] = f32x4{0, 0, 0, 0};
  {
    LAS unsigned char* ab = L + ch * CPY_CH + (lr & 3) * CPY + 2 * (2032 - 1792 - 192 + 8 * lq - 4 * (lr >> 2) - 32 * (w & 1));
    LAS unsigned char* ub = L + B_U + ch * U_CH + 512 + 2 * (16 * lr - 16 + 8 * lq - 192 - 32 * (w & 1));
    v4u afA[8], bfA[8], afB[8], bfB[8];
    auto ldc = [&](v4u (&af)[8], v4u (&bf)[8], int rr) {
#pragma unroll
      for (int i = 0; i < 8; ++i) {
        bf[i] = *(LAS v4u*)(ub + 128 * (3 - rr) + 512 * i);
        v2u x0, x1;
        asm volatile("" ::: "memory");
        x0 = *(LAS v2u*)(ab + 128 * (3 - rr) + 512 * (7 - i));
        asm volatile("" ::: "memory");
        x1 = *(LAS v2u*)(ab + 128 * (3 - rr) + 512 * (7 - i) + 8);
        af[i] = v4u{x0.x, x0.y, x1.x, x1.y};
      }
    };
    auto mmc = [&](v4u (&af)[8], v4u (&bf)[8]) {
#pragma unroll
      for (int sm = 0; sm < 8; ++sm)
#pragma unroll
        for (int o = 0; o <= sm; ++o) {
          if (sm + o < 8) acc[sm + o] = mfma_bf16(af[sm], bf[o], acc[sm + o]);
          if (o < sm && sm + o < 8) acc[sm + o] = mfma_bf16(af[o], bf[sm], acc[sm + o]);
        }
    };
    ldc(afA, bfA, 0);
    SB0();
#pragma unroll
    for (int rr = 0; rr < 4; rr += 2) {
      ldc(afB, bfB, rr + 1);
      mmc(afA, bfA);
#pragma unroll
      for (int i = 0; i < 24; ++i) { SGB(0x8, 1); SGB(0x100, 1); }
      SB0();
      if (rr + 2 < 4) ldc(afA, bfA, rr + 2);
      mmc(afB, bfB);
#pragma unroll
      for (int i = 0; i < 24; ++i) { SGB(0x8, 1); SGB(0x100, 1); }
      SB0();
    }
  }
  STAMP(8);
  v2u fl2;
  const unsigned f2vo = (unsigned)((4 * w + (lane & 3) + 16 * ((lane >> 2) & 1)) * 128);
  bld8(fl2, rs, f2vo, WS_F2 + g * 4096);
  {
    LAS unsigned char* X = L + 36864 + w * 4096;
    const int keep = (w & 1) * 4;
    f32x4 mine[4];
#pragma unroll
    for (int i = 0; i < 4; ++i) {
      f32x4 lo = acc[i], hi = acc[4 + i];
      asm("" : "+v"(lo), "+v"(hi));
      *(LAS f32x4*)(X + i * 1024 + lane * 16) = (w & 1) ? lo : hi;
      mine[i] = (w & 1) ? hi : lo;
    }
    BAR();
    LAS unsigned char* Y = L + 36864 + (w ^ 1) * 4096;
    v2u yo[4];
#pragma unroll
    for (int i = 0; i < 4; ++i) {
      const f32x4 s = mine[i] + *(LAS f32x4*)(Y + i * 1024 + lane * 16);
      yo[i].x = nosent(pk_bf16(s[0], s[1]));
      yo[i].y = nosent(pk_bf16(s[2], s[3]));
    }
    const unsigned dst = WS_YT + (d * 8 + keep) * 512;
    const unsigned vo = (unsigned)((lq & 1) * 512 + (lq >> 1) * 256 + lr * 16);
#pragma unroll
    for (int i = 0; i < 4; i += 2) {
      auto r0 = __builtin_amdgcn_permlane16_swap(yo[i].x, yo[i + 1].x, false, false);
      auto r1 = __builtin_amdgcn_permlane16_swap(yo[i].y, yo[i + 1].y, false, false);
      bst16<0>(v4u{r0[0], r1[0], r0[1], r1[1]}, rs, vo, dst + i * 512);
    }
  }
  STAMP(9);
  {
    const int c0 = tid >> 3, hh = (tid >> 2) & 1, l4 = tid & 3;
    const unsigned src = WS_YT + (64 * g * 8 + (m >> 2)) * 512 + (m & 3) * 64;
    asm volatile("s_waitcnt vmcnt(0)" : "+v"(fl2)::"memory");
    {
      unsigned sp = 0;
      while (!wall((((u64)fl2.y << 32) | fl2.x) == tag) && ++sp < SPIN_CAP) {
        __builtin_amdgcn_s_sleep(8);
        bld8(fl2, rs, f2vo, WS_F2 + g * 4096);
        asm volatile("s_waitcnt vmcnt(0)" : "+v"(fl2)::"memory");
      }
    }
    const unsigned vo = (unsigned)(c0 * 4096 + hh * 256 + l4 * 16);
    v4u a0, a1;
    unsigned spins = 0;
    for (;;) {
      bld16<0>(a0, rs, vo, src);
      bld16<0>(a1, rs, vo, src + 32 * 4096);
      asm volatile("s_waitcnt vmcnt(0)" : "+v"(a0), "+v"(a1)::"memory");
      if (!wany(has_sent(a0) || has_sent(a1)) || ++spins >= SPIN_CAP) break;
      __builtin_amdgcn_s_sleep(6);
    }
    STAMP(10);
    if (m == 0 && tid == 0) set_flag(p.ws, WS_EPB + 256 * g, tag);
    LAS unsigned char* q = L + c0 * 128 + l4 * 32 + hh * 16;
    *(LAS v4u*)q = a0;
    *(LAS v4u*)(q + 4096) = a1;
  }
  BAR();
  {
    LAS unsigned char* q = L + (8 * lq + ((lane >> 2) & 3)) * 128 + 32 * w + 8 * (lane & 3);
    GAS unsigned char* op = (GAS unsigned char*)(p.out + (size_t)(64 * m + 16 * w + lr) * 1024 + 512 + 64 * g + 8 * lq);
#pragma unroll
    for (int it = 0; it < 2; ++it) *(GAS v4u*)(op + it * 64) = tr_read2(q + it * 4096, q + it * 4096 + 512);
  }
  STAMP(11);
  STAMP_END(12);
}

__global__ void __launch_bounds__(256, 2) fused_kernel(Params p) {
  extern __shared__ __attribute__((aligned(16))) unsigned char smem[];
  LAS unsigned char* L = (LAS unsigned char*)smem;
  asm volatile("" ::"s"(p.x), "s"(p.wqkv), "s"(p.wo), "s"(p.u), "s"(p.lar), "s"(p.aim), "s"(p.cre), "s"(p.cim), "s"(p.ldt), "s"(p.dsk), "s"(p.out), "s"(p.ws));
  const unsigned char* code = (const unsigned char*)(__builtin_amdgcn_s_getpc() & ~(u64)255);
  STAMP_DECL
  STAMP(0);
  int bx = blockIdx.x;
  const int idx = blockIdx.x & 255;
  if (bx >= 256) roleA_first(p, L, idx);
  asm volatile("" : "+s"(bx));
  if (bx >= 256) roleA(p, L, idx STAMP_PASS);
  else { BIn in; roleB_first(p, idx, code, in); roleB(p, L, idx, in STAMP_PASS); }
}

#if defined(__HIP_DEVICE_COMPILE__)
#pragma clang attribute pop
#endif

extern "C" void kernel_launch(void* const* d_in, const int* in_sizes, int n_in, void* d_out, int out_size, void* d_ws,
                              size_t ws_size, hipStream_t stream) {
  Params p;
  p.x = (const u16*)d_in[0];
  p.wqkv = (const u16*)d_in[1];
  p.wo = (const u16*)d_in[2];
  p.u = (const u16*)d_in[3];
  p.lar = (const u16*)d_in[4];
  p.aim = (const u16*)d_in[5];
  p.cre = (const u16*)d_in[6];
  p.cim = (const u16*)d_in[7];
  p.ldt = (const u16*)d_in[8];
  p.dsk = (const u16*)d_in[9];
  p.out = (u16*)d_out;
  p.ws = (unsigned char*)d_ws;
  (void)hipFuncSetAttribute((const void*)fused_kernel, hipFuncAttributeMaxDynamicSharedMemorySize, LDS_BYTES);
  fused_kernel<<<512, 256, LDS_BYTES, stream>>>(p);
}
```
